```python
import torch

def _softmax(x, axis=-1):
    return torch.softmax(x, dim=axis)
def _gelu(x):
    return torch.nn.functional.gelu(x, approximate="tanh")
def _silu(x):
    return torch.nn.functional.silu(x)
def _sigmoid(x):
    return torch.sigmoid(x)
def _layernorm(x, axis=-1, eps=1e-5):
    m = x.mean(dim=axis, keepdim=True)
    v = x.var(dim=axis, keepdim=True, unbiased=False)
    return (x - m) / torch.sqrt(v + eps)
def _scatter_add(tgt, idx, src, axis=0):
    return tgt.clone().index_add_(axis, idx, src)
def _segment_sum(src, seg, n):
    out = torch.zeros((n,) + src.shape[1:], dtype=src.dtype)
    return out.index_add_(0, seg, src)
def _pad(x, pw):
    flat = tuple(v for pair in reversed(pw) for v in pair)
    return torch.nn.functional.pad(x, flat)

kernel_name = 'recurrent_scan__ssm_conv_s5__mixer_s_mlp__prompt'

def setup_inputs():

    _w = (lambda a: (lambda t: t.to(torch.bfloat16) if t.is_floating_point() else t)(torch.as_tensor(a)))
    def _A_setup():
        B, S, D = 1, 2048, 512
        P = D
        Lambda_re = -0.5 * (lambda _x: torch.exp(torch.as_tensor(_x)))((lambda *s: torch.randn(*s, dtype=torch.bfloat16) * 0.2)(P))
        Lambda_im = (lambda *s: torch.randn(*s, dtype=torch.bfloat16) * 0.4)(P) * 1.0
        B_re = (lambda *s: torch.randn(*s, dtype=torch.bfloat16) * 0.2)(P, D) * (D ** -0.5)
        B_im = (lambda *s: torch.randn(*s, dtype=torch.bfloat16) * 0.2)(P, D) * (D ** -0.5)
        C_re = (lambda *s: torch.randn(*s, dtype=torch.bfloat16) * 0.2)(D, P) * (P ** -0.5)
        C_im = (lambda *s: torch.randn(*s, dtype=torch.bfloat16) * 0.2)(D, P) * (P ** -0.5)
        log_dt = (lambda *s: torch.randn(*s, dtype=torch.bfloat16) * 0.2)(P) * 0.3 - 2.0
        Dskip = (lambda *s: torch.randn(*s, dtype=torch.bfloat16) * 0.05)(D)
        return {
            "x": _w((lambda *s: torch.randn(*s, dtype=torch.bfloat16) * 0.05)(B, S, D)),
            "Lambda_re": _w(Lambda_re),
            "Lambda_im": _w(Lambda_im),
            "B_re": _w(B_re),
            "B_im": _w(B_im),
            "C_re": _w(C_re),
            "C_im": _w(C_im),
            "log_dt": _w(log_dt),
            "Dskip": _w(Dskip),
        }
    _A_in = _A_setup()

    def _B_setup():
        B, S, D, F = 1, 2048, 512, 1792
        return {
            "x":  _w((lambda *s: torch.randn(*s, dtype=torch.bfloat16) * 0.05)(B, S, D)),
            "w1": _w((lambda *s: torch.randn(*s, dtype=torch.bfloat16) * 3.2)(D, D) * (D ** -0.5)),
            "w2": _w((lambda *s: torch.randn(*s, dtype=torch.bfloat16) * 3.2)(D, D) * (D ** -0.5)),
            "w3": _w((lambda *s: torch.randn(*s, dtype=torch.bfloat16) * 3.2)(D, F) * (D ** -0.5)),
            "w4": _w((lambda *s: torch.randn(*s, dtype=torch.bfloat16) * 3.2)(F, D) * (F ** -0.5)),
        }
    _B_in = _B_setup()

    return {
        "A_x": _A_in["x"],
        "A_Lambda_re": _A_in["Lambda_re"],
        "A_Lambda_im": _A_in["Lambda_im"],
        "A_B_re": _A_in["B_re"],
        "A_B_im": _A_in["B_im"],
        "A_C_re": _A_in["C_re"],
        "A_C_im": _A_in["C_im"],
        "A_log_dt": _A_in["log_dt"],
        "A_Dskip": _A_in["Dskip"],
        "B_x": _B_in["x"],
        "B_w1": _B_in["w1"],
        "B_w2": _B_in["w2"],
        "B_w3": _B_in["w3"],
        "B_w4": _B_in["w4"],
    }

def reference(A_x, A_Lambda_re, A_Lambda_im, A_B_re, A_B_im, A_C_re, A_C_im, A_log_dt, A_Dskip, B_x, B_w1, B_w2, B_w3, B_w4):
    def _A_ref(x, Lambda_re, Lambda_im, B_re, B_im, C_re, C_im, log_dt, Dskip):
        Bsz, L, H = x.shape
        Lam = Lambda_re + 1j * Lambda_im
        Bmat = B_re + 1j * B_im
        Cmat = C_re + 1j * C_im
        dt = torch.exp(log_dt)
        Lam_bar = torch.exp(dt * Lam)
        B_bar = ((Lam_bar - 1.0) / Lam)[:, None] * Bmat
        Bu = torch.einsum("bld,pd->blp", x + 0j, B_bar)
        h = torch.zeros_like(Bu[:, 0, :])
        hs = []
        for t in range(L):
            h = Lam_bar[None, :] * h + Bu[:, t, :]
            hs.append(h)
        hseq = torch.stack(hs, 1)
        y = 2.0 * torch.einsum("blp,dp->bld", hseq, Cmat).real + x * Dskip[None, None, :]
        return y

    def _B_ref(x, w1, w2, w3, w4):
        B, S, D = x.shape
        h = _gelu(_layernorm(x, axis=-1) @ w1)
        c = D // 4
        g0 = _pad(h[:, :, 0*c:1*c], ((0, 0), (1, 0), (0, 0)))[:, :S, :]
        g1 = _pad(h[:, :, 1*c:2*c], ((0, 0), (0, 1), (0, 0)))[:, 1:, :]
        g2 = _pad(h[:, :, 2*c:3*c], ((0, 0), (2, 0), (0, 0)))[:, :S, :]
        g3 = _pad(h[:, :, 3*c:   ], ((0, 0), (0, 2), (0, 0)))[:, 2:, :]
        hs = torch.concatenate([g0, g1, g2, g3], axis=-1) @ w2
        x1 = x + hs
        h2 = _gelu(_layernorm(x1, axis=-1) @ w3) @ w4
        return x1 + h2

    x = A_x
    xn = _layernorm(x, axis=-1)
    s = _A_ref(xn, A_Lambda_re, A_Lambda_im, A_B_re, A_B_im, A_C_re, A_C_im, A_log_dt, A_Dskip)
    g = _silu(_B_ref(xn, B_w1, B_w2, B_w3, B_w4))
    return x + s * g
```

```cpp
#include <hip/hip_runtime.h>
#include <hip/hip_bf16.h>
#include <stdint.h>

typedef __attribute__((ext_vector_type(4))) short s16x4;
typedef __attribute__((ext_vector_type(8))) short s16x8;
typedef __attribute__((ext_vector_type(4))) float f32x4;
typedef __attribute__((ext_vector_type(2))) float f32x2;
typedef __attribute__((ext_vector_type(4))) unsigned u32x4;
typedef __attribute__((ext_vector_type(2))) unsigned u32x2;
typedef __attribute__((ext_vector_type(2))) __bf16 bf16x2v;
typedef unsigned long long u64;
#define LAS __attribute__((address_space(3)))
#define GAS __attribute__((address_space(1)))
#define DEVI __device__ __forceinline__

namespace {
constexpr int SEQ = 2048, DM = 512, FF = 1792;
constexpr float EPS = 1e-5f;
constexpr unsigned WS_EPOCH = 0, WS_TMO = 2048;
constexpr unsigned WS_F1 = 4096, WS_F2 = 8192, WS_F3 = 12288, WS_F4 = 16384, WS_F5 = 20480;
constexpr unsigned WS_H1S = 1u << 20;
constexpr unsigned WS_UT = WS_H1S + (2u << 20);
constexpr unsigned WS_X1 = WS_UT + (4u << 20);
constexpr unsigned WS_ST3 = WS_X1 + (2u << 20);
constexpr unsigned WS_HT = WS_ST3 + (1u << 20);
constexpr unsigned WS_HID = WS_HT + (4u << 20);
constexpr unsigned WS_END = WS_HID + (8u << 20);
constexpr int SLOT = 16384;
constexpr int L_XN = 4 * SLOT;
constexpr int L_STG = 8 * SLOT;
constexpr int L_MISC = L_STG + 8 * 2048;
constexpr int L_ST1 = L_MISC;
constexpr int L_ST5 = L_ST1 + 768;
constexpr int L_SCN = L_ST5 + 1024;
constexpr int L_CNT = L_SCN + 64;
constexpr int L_TAG = L_CNT + 32;
constexpr int LDS_BYTES = L_TAG + 32;
constexpr int NSTAGE = 82;
constexpr unsigned SPIN_CAP = 1u << 16;

struct Params {
  const char *x, *lam_re, *lam_im, *b_re, *b_im, *c_re, *c_im, *log_dt, *dskip, *w1, *w2, *w3, *w4;
  float* out;
  char* ws;
};

DEVI float as_f(unsigned u) { return __builtin_bit_cast(float, u); }
DEVI float bf_lo(unsigned u) { return __builtin_bit_cast(float, u << 16); }
DEVI float bf_hi(unsigned u) { return __builtin_bit_cast(float, u & 0xffff0000u); }
DEVI unsigned pk_bf16(float a, float b) {
  f32x2 p = {a, b};
  return __builtin_bit_cast(unsigned, __builtin_convertvector(p, bf16x2v));
}
DEVI float ld_bf16(const char* p, int i) {
  return __builtin_bit_cast(float, (unsigned)(*(const unsigned short*)(p + 2 * i)) << 16);
}
DEVI float sigmoidf_(float z) { return __builtin_amdgcn_rcpf(1.0f + __expf(-z)); }
DEVI float gelu_tanh(float v) {
  const float u2 = 1.5957691216057308f * (v + 0.044715f * v * v * v);
  return v * sigmoidf_(u2);
}
DEVI void bar() { asm volatile("s_waitcnt lgkmcnt(0)\n\ts_barrier" ::: "memory"); }
DEVI f32x4 mfma(s16x8 a, s16x8 b, f32x4 c) { return __builtin_amdgcn_mfma_f32_16x16x32_bf16(a, b, c, 0, 0, 0); }
DEVI s16x8 lds_b128(LAS char* p) { return *(LAS s16x8*)p; }
DEVI s16x8 lds_tr2(LAS char* p0, LAS char* p1) {
  s16x4 a = __builtin_amdgcn_ds_read_tr16_b64_v4i16((LAS s16x4*)p0);
  s16x4 b = __builtin_amdgcn_ds_read_tr16_b64_v4i16((LAS s16x4*)p1);
  return __builtin_shufflevector(a, b, 0, 1, 2, 3, 4, 5, 6, 7);
}
DEVI int hswz(int k) { return ((k >> 1) & 1) | ((((k >> 2) ^ (k >> 3)) & 1) << 1); }

template <int AUX>
DEVI void glds(const char* base, unsigned voff, LAS char* dst) {
  __builtin_amdgcn_global_load_lds((const GAS unsigned*)(base + voff), (LAS unsigned*)dst, 16, 0, AUX);
}
DEVI void wait_vm(int n) {
  switch (n >> 2) {
    case 0: asm volatile("s_waitcnt vmcnt(0)" ::: "memory"); break;
    case 1: asm volatile("s_waitcnt vmcnt(4)" ::: "memory"); break;
    case 2: asm volatile("s_waitcnt vmcnt(8)" ::: "memory"); break;
    case 3: asm volatile("s_waitcnt vmcnt(12)" ::: "memory"); break;
    case 4: asm volatile("s_waitcnt vmcnt(16)" ::: "memory"); break;
    case 5: asm volatile("s_waitcnt vmcnt(20)" ::: "memory"); break;
    case 6: asm volatile("s_waitcnt vmcnt(24)" ::: "memory"); break;
    case 7: asm volatile("s_waitcnt vmcnt(28)" ::: "memory"); break;
    default: asm volatile("s_waitcnt vmcnt(32)" ::: "memory"); break;
  }
}
DEVI u64 ld_flag(const char* p) {
  return __hip_atomic_load((const GAS u64*)p, __ATOMIC_RELAXED, __HIP_MEMORY_SCOPE_AGENT);
}
template <int NL>
DEVI void wait_flags(char* ws, unsigned off, int n, u64 tag, int lane) {
  const char* fp = ws + off + 8 * (NL > 1 || lane < n ? lane : 0);
#pragma unroll 1
  for (unsigned spins = 0;; ++spins) {
    u64 v[NL];
#pragma unroll
    for (int i = 0; i < NL; ++i) v[i] = ld_flag(fp + 512 * i);
    bool ok = true;
#pragma unroll
    for (int i = 0; i < NL; ++i) ok &= (v[i] == tag);
    if (__all(ok)) break;
    __builtin_amdgcn_s_sleep(1);
    if ((spins & 63u) == 63u && ld_flag(ws + WS_TMO) == tag) break;
    if (spins > SPIN_CAP) {
      __hip_atomic_store((GAS u64*)(ws + WS_TMO), tag, __ATOMIC_RELAXED, __HIP_MEMORY_SCOPE_AGENT);
      break;
    }
  }
  __builtin_amdgcn_fence(__ATOMIC_ACQUIRE, "wavefront");
  asm volatile("" ::: "memory");
}
DEVI void publish(LAS char* lds, int which, char* ws, unsigned flag_off, int lane) {
  asm volatile("s_waitcnt vmcnt(0)" ::: "memory");
  if (lane == 0) {
    const unsigned old = __hip_atomic_fetch_add((LAS unsigned*)(lds + L_CNT) + which, 1u, __ATOMIC_RELAXED,
                                                __HIP_MEMORY_SCOPE_WORKGROUP);
    if (old == 7u) {
      const u64 tag = *(volatile LAS u64*)(lds + L_TAG);
      __hip_atomic_store((GAS u64*)(ws + flag_off), tag, __ATOMIC_RELAXED, __HIP_MEMORY_SCOPE_AGENT);
    }
  }
}
DEVI int slot_of(int u) { return (u < 12 ? (u & 3) : ((u - 12) & 7)) * SLOT; }

DEVI void loader(const Params& p, LAS char* lds, int lw, int lane, int r, int c, int R5, int c5, bool do5) {
  const bool isA = lw < 2;
  const int half = lw & 1;
  unsigned vK1[2], vK4[2], vM[2], vW3[2];
#pragma unroll
  for (int e = 0; e < 2; ++e) {
    const int row = 8 * e + (lane >> 3), ch = lane & 7;
    const int sk = ((((ch >> 1) ^ hswz(row)) << 1) | (ch & 1)) * 16;
    vK1[e] = row * 1024 + sk;
    vK4[e] = row * 4096 + sk;
    vM[e] = row * 1024 + ((ch ^ ((row >> 1) & 7)) * 16);
    const int row3 = 4 * e + (lane >> 4), ch3 = lane & 15;
    vW3[e] = row3 * (FF * 2) + (((((ch3 >> 1) ^ (row3 & 7)) << 1) | (ch3 & 1)) * 16);
  }
  const unsigned vL = lane * 16;
  char* const ws = p.ws;
  const int skip = do5 ? 0 : 16, npos = NSTAGE - skip;
  const int ch0 = 2 * (int)blockIdx.x;
  u64 tag = 0;
  int nxt = 0, issued = 0;
  for (int j = 0; j < npos; ++j) {
    int lim = j <= 12 ? (j + 2 < 3 ? 3 : j + 2) : j + 6;
    if (lim > npos - 1) lim = npos - 1;
    if (isA) {
      const int sj = j < 22 ? j : j + skip;
      const int end = sj < 12 ? 11 : sj < 20 ? 19 : sj < 22 ? 21 : sj < 38 ? 37 : sj < 54 ? 53 - skip : 81 - skip;
      if (lim > end) lim = end;
      if (sj == 12) {
        tag = *(volatile LAS u64*)(lds + L_TAG);
        const int r0 = r > 0 ? r - 1 : 0, r1 = r < 31 ? r + 1 : 31;
        wait_flags<1>(ws, WS_F1 + r0 * 64, (r1 - r0 + 1) * 8, tag, lane);
      } else if (sj == 20) {
        wait_flags<1>(ws, WS_F2 + (ch0 >> 6) * 256, 32, tag, lane);
      } else if (sj == 22) {
        wait_flags<1>(ws, WS_F3 + R5 * 128, 16, tag, lane);
      } else if (sj == 38) {
        wait_flags<4>(ws, WS_F4, 256, tag, lane);
        if (blockIdx.x == 0 && lw == 0 && lane == 0)
          __hip_atomic_store((GAS u64*)(ws + WS_EPOCH), tag, __ATOMIC_RELAXED, __HIP_MEMORY_SCOPE_AGENT);
        asm volatile("s_waitcnt vmcnt(0)" ::: "memory");
      } else if (sj == 54) {
        wait_flags<1>(ws, WS_F5 + (r >> 1) * 128, 14, tag, lane);
      }
    }
    for (; nxt <= lim; ++nxt) {
      const int s = nxt < 22 ? nxt : nxt + skip;
      LAS char* dst = lds + slot_of(nxt) + lw * 4096;
      if (s < 4) {
        const char* b = p.w1 + (128 * s + 32 * lw) * 1024 + c * 128;
#pragma unroll
        for (int i = 0; i < 4; ++i) glds<0>(b + (i >> 1) * 16384, vK1[i & 1], dst + i * 1024);
        issued += 4;
      } else if (s < 12) {
        const char* b = ((lw >> 1) ? p.b_im : p.b_re) + (64 * c + 32 * half) * 1024 + (s - 4) * 128;
#pragma unroll
        for (int i = 0; i < 4; ++i) glds<0>(b + (i >> 1) * 16384, vM[i & 1], dst + i * 1024);
        issued += 4;
      } else if (s < 20) {
        if (isA) {
          const char* b = ws + WS_H1S + (r * 16 + 2 * (s - 12) + half) * 4096;
#pragma unroll
          for (int i = 0; i < 4; ++i) glds<16>(b + i * 1024, vL, dst + i * 1024);
        } else {
          const char* b = p.w2 + (64 * (s - 12) + 32 * half) * 1024 + c * 128;
#pragma unroll
          for (int i = 0; i < 4; ++i) glds<0>(b + (i >> 1) * 16384, vK1[i & 1], dst + i * 1024);
        }
        issued += 4;
      } else if (s < 22) {
        if (isA && s == 20) {
          const char* b = ws + WS_UT + (ch0 + lw) * 4096;
          LAS char* d2 = lds + slot_of(nxt) + lw * 8192;
#pragma unroll
          for (int i = 0; i < 8; ++i) glds<16>(b + (i >> 2) * (512 * 4096) + (i & 3) * 1024, vL, d2 + i * 1024);
          issued += 8;
        }
      } else if (s < 38) {
        if (isA) {
          const char* b = ws + WS_X1 + (R5 * 16 + (s - 22)) * 8192 + half * 4096;
#pragma unroll
          for (int i = 0; i < 4; ++i) glds<16>(b + i * 1024, vL, dst + i * 1024);
        } else {
          const char* b = p.w3 + (32 * (s - 22) + 16 * half) * (FF * 2) + c5 * 256;
#pragma unroll
          for (int i = 0; i < 4; ++i) glds<0>(b + (i >> 1) * (8 * FF * 2), vW3[i & 1], dst + i * 1024);
        }
        issued += 4;
      } else if (s < 54) {
        const int q = s - 38;
        if (isA) {
          const char* b = ws + WS_HT + (64 * q + 32 * half) * 4096 + r * 128;
#pragma unroll
          for (int i = 0; i < 4; ++i) glds<16>(b + (i >> 1) * (16 * 4096), vK4[i & 1], dst + i * 1024);
        } else {
          const char* b = (q < 8 ? p.c_re : p.c_im) + (64 * c + 32 * half) * 1024 + (q & 7) * 128;
#pragma unroll
          for (int i = 0; i < 4; ++i) glds<0>(b + (i >> 1) * 16384, vM[i & 1], dst + i * 1024);
        }
        issued += 4;
      } else {
        const int q = s - 54;
        if (isA) {
          const char* b = ws + WS_HID + (r * 56 + 2 * q + half) * 4096;
#pragma unroll
          for (int i = 0; i < 4; ++i) glds<16>(b + i * 1024, vL, dst + i * 1024);
        } else {
          const char* b = p.w4 + (64 * q + 32 * half) * 1024 + c * 128;
#pragma unroll
          for (int i = 0; i < 4; ++i) glds<0>(b + (i >> 1) * 16384, vK1[i & 1], dst + i * 1024);
        }
        issued += 4;
      }
    }
    const int need = isA ? (j == 20 ? 88 : 4 * (j + 1)) : (j < 20 ? 4 * (j + 1) : j < 22 ? 80 : 4 * (j + 1) - 8);
    wait_vm(issued - need);
    asm volatile("s_barrier" ::: "memory");
  }
}

DEVI float wave_sum(float v) {
#pragma unroll
  for (int o = 32; o >= 1; o >>= 1) v += __shfl_xor(v, o);
  return v;
}
DEVI f32x2 cmul(f32x2 a, f32x2 b) {
  f32x2 r = {a[0] * b[0] - a[1] * b[1], a[0] * b[1] + a[1] * b[0]};
  return r;
}
DEVI f32x2 cfma(f32x2 a, f32x2 b, f32x2 c) {
  f32x2 r = {fmaf(a[0], b[0], fmaf(-a[1], b[1], c[0])), fmaf(a[0], b[1], fmaf(a[1], b[0], c[1]))};
  return r;
}

DEVI void compute(const Params& p, LAS char* lds, int cw, int lane, int r, int c, int R5, int c5, bool do5) {
  const int fr = lane & 15, q = lane >> 4;
  char* const ws = p.ws;
  const auto rs = __builtin_amdgcn_make_buffer_rsrc(ws, 0, (int)WS_END, 0x00020000);
  const int np = cw & 1, tt = cw >> 1;
  const int tok = 64 * r + 16 * tt + fr;
  const int col0 = 64 * c + 32 * np + 4 * q;

  u32x4 xr[8];
#pragma unroll
  for (int i = 0; i < 8; ++i) xr[i] = *(const u32x4*)(p.x + (64 * r + 8 * cw + i) * 1024 + lane * 16);
  u32x2 xq[2];
#pragma unroll
  for (int i = 0; i < 2; ++i) xq[i] = *(const u32x2*)(p.x + tok * 1024 + (col0 + 16 * i) * 2);
  if (cw == 0 && lane == 0) {
    const u64 e = ld_flag(ws + WS_EPOCH);
    *(LAS u64*)(lds + L_TAG) = e + 1ull;
  }
  if (cw == 1 && lane < 8) *((LAS unsigned*)(lds + L_CNT) + lane) = 0u;
#pragma unroll
  for (int i = 0; i < 8; ++i) {
    float v[8];
#pragma unroll
    for (int k = 0; k < 4; ++k) { v[2 * k] = bf_lo(xr[i][k]); v[2 * k + 1] = bf_hi(xr[i][k]); }
    float s = ((v[0] + v[1]) + (v[2] + v[3])) + ((v[4] + v[5]) + (v[6] + v[7]));
    const float mean = wave_sum(s) * (1.0f / DM);
    float m2 = 0.f;
#pragma unroll
    for (int k = 0; k < 8; ++k) { v[k] -= mean; m2 = fmaf(v[k], v[k], m2); }
    const float var = wave_sum(m2) * (1.0f / DM);
    const float rstd = rsqrtf(var + EPS);
    u32x4 o;
#pragma unroll
    for (int k = 0; k < 4; ++k) o[k] = pk_bf16(v[2 * k] * rstd, v[2 * k + 1] * rstd);
    const int row = 8 * cw + i;
    *(LAS u32x4*)(lds + L_XN + row * 1024 + ((lane ^ (row & 15)) * 16)) = o;
    if (lane == 0) {
      LAS float* st = (LAS float*)(lds + L_ST1);
      st[row] = mean;
      st[64 + row] = rstd;
      st[128 + row] = rsqrtf(var * rstd * rstd + EPS);
    }
  }

  const int tq = fr >> 2, tp = fr & 3;
  int trN[2][2], trP[2][2], trT[2];
#pragma unroll
  for (int t = 0; t < 2; ++t) {
    const int rn = 8 * q + 4 * t + tq, rp = 16 * t + 4 * q + tq;
#pragma unroll
    for (int i = 0; i < 2; ++i) {
      trN[i][t] = rn * 128 + (((2 * np + i) ^ hswz(rn)) * 32) + tp * 8;
      trP[i][t] = rp * 128 + (((2 * np + i) ^ hswz(rp)) * 32) + tp * 8;
    }
    trT[t] = rn * 128 + ((tt ^ hswz(rn)) * 32) + tp * 8;
  }
  const int xsw = (q ^ fr) * 16;
  float mean_t, rstd_t;

  {
    f32x4 acc[2] = {};
#pragma unroll 1
    for (int u = 0; u < 4; ++u) {
      bar();
      LAS char* sl = lds + slot_of(u);
      LAS char* xa = lds + L_XN + (16 * tt + fr) * 1024 + u * 256;
#pragma unroll
      for (int ks = 0; ks < 4; ++ks) {
        const s16x8 xb = lds_b128(xa + ((64 * ks) ^ xsw));
#pragma unroll
        for (int i = 0; i < 2; ++i)
          acc[i] = mfma(lds_tr2(sl + ks * 4096 + trN[i][0], sl + ks * 4096 + trN[i][1]), xb, acc[i]);
      }
    }
    LAS float* st = (LAS float*)(lds + L_ST1);
    mean_t = st[16 * tt + fr];
    rstd_t = st[64 + 16 * tt + fr];
    const float r2 = st[128 + 16 * tt + fr];
    u32x4 o;
#pragma unroll
    for (int i = 0; i < 2; ++i) {
      o[2 * i] = pk_bf16(gelu_tanh(acc[i][0] * r2), gelu_tanh(acc[i][1] * r2));
      o[2 * i + 1] = pk_bf16(gelu_tanh(acc[i][2] * r2), gelu_tanh(acc[i][3] * r2));
    }
    const int grp = c >> 1, dl = (grp & 1) ? -(1 + (grp >> 1)) : (1 + (grp >> 1));
    const int ks32 = 2 * c + np;
    const int td = tok + dl;
    if (td >= 0 && td < SEQ)
      __builtin_amdgcn_raw_buffer_store_b128(
          o, rs, WS_H1S + ((td >> 6) * 16 + ks32) * 4096 + ((td >> 4) & 3) * 1024 + (q * 16 + (td & 15)) * 16, 0, 16);
    if (tok - dl < 0 || tok - dl >= SEQ) {
      const u32x4 z = {0u, 0u, 0u, 0u};
      __builtin_amdgcn_raw_buffer_store_b128(
          z, rs, WS_H1S + ((tok >> 6) * 16 + ks32) * 4096 + ((tok >> 4) & 3) * 1024 + (q * 16 + (tok & 15)) * 16, 0, 16);
    }
    publish(lds, 0, ws, WS_F1 + (r * 8 + c) * 8, lane);
  }

  {
    const int th = cw & 1, pq = cw >> 1;
    f32x4 acc[2][2] = {};
    int bo[2];
#pragma unroll
    for (int nt = 0; nt < 2; ++nt) {
      const int row = 32 * pq + 16 * nt + fr;
      bo[nt] = row * 128 + ((q ^ ((row >> 1) & 7)) * 16);
    }
#pragma unroll 1
    for (int u = 4; u < 12; ++u) {
      bar();
      LAS char* sl = lds + slot_of(u);
      const int s2 = u - 4;
      LAS char* xa = lds + L_XN + (32 * th + fr) * 1024 + (s2 >> 1) * 256;
#pragma unroll
      for (int ks = 0; ks < 2; ++ks) {
        const int xo = (128 * (s2 & 1) + 64 * ks) ^ xsw;
        const s16x8 a0 = lds_b128(xa + xo), a1 = lds_b128(xa + 16384 + xo);
#pragma unroll
        for (int nt = 0; nt < 2; ++nt) {
          const s16x8 b = lds_b128(sl + (bo[nt] ^ (64 * ks)));
          acc[0][nt] = mfma(a0, b, acc[0][nt]);
          acc[1][nt] = mfma(a1, b, acc[1][nt]);
        }
      }
    }
    LAS char* stg = lds + L_STG + cw * 2048;
#pragma unroll
    for (int nt = 0; nt < 2; ++nt)
#pragma unroll
      for (int mt = 0; mt < 2; ++mt) {
        const int row = 16 * nt + fr;
        const u32x2 v = {pk_bf16(acc[mt][nt][0], acc[mt][nt][1]), pk_bf16(acc[mt][nt][2], acc[mt][nt][3])};
        *(LAS u32x2*)(stg + row * 64 + (((4 * mt + q) ^ (((row >> 1) & 3) << 1)) * 8)) = v;
      }
#pragma unroll
    for (int ii = 0; ii < 2; ++ii) {
      const int row = 16 * ii + (lane >> 2), pr = lane & 3;
      const u32x4 v = *(LAS u32x4*)(stg + row * 64 + ((pr ^ ((row >> 1) & 3)) * 16));
      const int prow = (pq >> 1) * 512 + 64 * c + 32 * (pq & 1) + row;
      __builtin_amdgcn_raw_buffer_store_b128(v, rs, WS_UT + prow * 4096 + (64 * r + 32 * th) * 2 + pr * 16, 0, 16);
    }
    publish(lds, 1, ws, WS_F2 + (c * 32 + r) * 8, lane);
  }

  u32x4 x1p;
  {
    f32x4 acc[2] = {};
#pragma unroll 1
    for (int u = 12; u < 20; ++u) {
      bar();
      LAS char* sl = lds + slot_of(u);
#pragma unroll
      for (int ks = 0; ks < 2; ++ks) {
        const s16x8 hb = lds_b128(sl + ks * 4096 + tt * 1024 + lane * 16);
#pragma unroll
        for (int i = 0; i < 2; ++i)
          acc[i] = mfma(lds_tr2(sl + 8192 + ks * 4096 + trP[i][0], sl + 8192 + ks * 4096 + trP[i][1]), hb, acc[i]);
      }
    }
#pragma unroll
    for (int i = 0; i < 2; ++i) {
      x1p[2 * i] = pk_bf16((bf_lo(xq[i][0]) - mean_t) * rstd_t + acc[i][0], (bf_hi(xq[i][0]) - mean_t) * rstd_t + acc[i][1]);
      x1p[2 * i + 1] = pk_bf16((bf_lo(xq[i][1]) - mean_t) * rstd_t + acc[i][2], (bf_hi(xq[i][1]) - mean_t) * rstd_t + acc[i][3]);
    }
    const int ks32 = 2 * c + np;
    __builtin_amdgcn_raw_buffer_store_b128(
        x1p, rs, WS_X1 + ((tok >> 7) * 16 + ks32) * 8192 + ((tok >> 4) & 7) * 1024 + lane * 16, 0, 16);
    float v[8];
#pragma unroll
    for (int k = 0; k < 4; ++k) { v[2 * k] = bf_lo(x1p[k]); v[2 * k + 1] = bf_hi(x1p[k]); }
    float s = ((v[0] + v[1]) + (v[2] + v[3])) + ((v[4] + v[5]) + (v[6] + v[7]));
    s += __shfl_xor(s, 16);
    s += __shfl_xor(s, 32);
    const float m32 = s * (1.0f / 32.0f);
    float m2 = 0.f;
#pragma unroll
    for (int k = 0; k < 8; ++k) { const float d = v[k] - m32; m2 = fmaf(d, d, m2); }
    m2 += __shfl_xor(m2, 16);
    m2 += __shfl_xor(m2, 32);
    if (q == 0) {
      const u32x2 sv = {__builtin_bit_cast(unsigned, m32), __builtin_bit_cast(unsigned, m2)};
      __builtin_amdgcn_raw_buffer_store_b64(sv, rs, WS_ST3 + (ks32 * SEQ + tok) * 8, 0, 16);
    }
    publish(lds, 2, ws, WS_F3 + (r * 8 + c) * 8, lane);
  }

  {
    const int chl = cw >> 2, wv = cw & 3, ch = 2 * (int)blockIdx.x + chl;
    const float lre = ld_bf16(p.lam_re, ch), lim = ld_bf16(p.lam_im, ch);
    const float dt = bf_lo(pk_bf16(expf(ld_bf16(p.log_dt, ch)), 0.f));
    float sn, cs;
    sincosf(dt * lim, &sn, &cs);
    const float mag = expf(dt * lre);
    const f32x2 a = {mag * cs, mag * sn};
    const float den = 1.0f / (lre * lre + lim * lim);
    const f32x2 am1 = {a[0] - 1.0f, a[1]}, linv = {lre * den, -lim * den};
    const f32x2 coef = cmul(am1, linv);
    f32x2 pw[7];
    {
      f32x2 t = cmul(a, a);
      t = cmul(t, t);
      pw[0] = cmul(t, t);
#pragma unroll
      for (int i = 1; i < 7; ++i) pw[i] = cmul(pw[i - 1], pw[i - 1]);
    }
    f32x2 pl = {1.0f, 0.0f};
#pragma unroll
    for (int i = 0; i < 6; ++i) {
      const f32x2 m = cmul(pl, pw[i]);
      if ((lane >> i) & 1) pl = m;
    }
    bar();
    LAS char* sl = lds + slot_of(20) + chl * 8192 + wv * 1024 + lane * 16;
    const u32x4 ur = *(LAS u32x4*)sl, ui = *(LAS u32x4*)(sl + 4096);
    f32x2 v[8];
#pragma unroll
    for (int k = 0; k < 4; ++k) {
      v[2 * k] = f32x2{bf_lo(ur[k]), bf_lo(ui[k])};
      v[2 * k + 1] = f32x2{bf_hi(ur[k]), bf_hi(ui[k])};
    }
    f32x2 T = v[0];
#pragma unroll
    for (int k = 1; k < 8; ++k) T = cfma(a, T, v[k]);
#pragma unroll
    for (int i = 0; i < 6; ++i) {
      f32x2 o = {__shfl_up(T[0], 1 << i), __shfl_up(T[1], 1 << i)};
      if (lane < (1 << i)) o = f32x2{0.f, 0.f};
      T = cfma(pw[i], o, T);
    }
    if (lane == 63) *(LAS f32x2*)(lds + L_SCN + cw * 8) = T;
    f32x2 P = {__shfl_up(T[0], 1), __shfl_up(T[1], 1)};
    if (lane == 0) P = f32x2{0.f, 0.f};
    bar();
    f32x2 cin = {0.f, 0.f};
#pragma unroll
    for (int w = 0; w < 3; ++w) {
      const f32x2 tw = *(LAS f32x2*)(lds + L_SCN + (4 * chl + w) * 8);
      if (w < wv) cin = cfma(pw[6], cin, tw);
    }
    P = cfma(pl, cin, P);
    float hr[8], hi[8];
#pragma unroll
    for (int k = 0; k < 8; ++k) {
      P = cfma(a, P, v[k]);
      const f32x2 h = cmul(coef, P);
      hr[k] = 2.0f * h[0];
      hi[k] = -2.0f * h[1];
    }
    u32x4 orr, oi;
#pragma unroll
    for (int k = 0; k < 4; ++k) { orr[k] = pk_bf16(hr[2 * k], hr[2 * k + 1]); oi[k] = pk_bf16(hi[2 * k], hi[2 * k + 1]); }
    __builtin_amdgcn_raw_buffer_store_b128(orr, rs, WS_HT + ch * 4096 + wv * 1024 + lane * 16, 0, 16);
    __builtin_amdgcn_raw_buffer_store_b128(oi, rs, WS_HT + (512 + ch) * 4096 + wv * 1024 + lane * 16, 0, 16);
    publish(lds, 3, ws, WS_F4 + (int)blockIdx.x * 8, lane);
  }

  int pos = 22;
  if (do5) {
    const int n5 = cw & 3, th = cw >> 2;
    int tr5[2][2];
#pragma unroll
    for (int t = 0; t < 2; ++t) {
      const int rp = 16 * t + 4 * q + tq;
#pragma unroll
      for (int i = 0; i < 2; ++i) tr5[i][t] = 8192 + rp * 256 + (((2 * n5 + i) ^ (rp & 7)) * 32) + tp * 8;
    }
    f32x4 acc[2][4] = {};
    f32x4 cs[2] = {};
    const s16x8 ones = {0x3f80, 0x3f80, 0x3f80, 0x3f80, 0x3f80, 0x3f80, 0x3f80, 0x3f80};
#pragma unroll 1
    for (int u = 22; u < 38; ++u) {
      bar();
      if (u == 22) {
        const int row = 128 * R5 + 16 * cw + fr;
        float mm[4], m2 = 0.f, ms = 0.f;
#pragma unroll
        for (int k = 0; k < 4; ++k) {
          const u32x2 sv = __builtin_amdgcn_raw_buffer_load_b64(rs, WS_ST3 + ((4 * q + k) * SEQ + row) * 8, 0, 16);
          mm[k] = as_f(sv[0]);
          m2 += as_f(sv[1]);
          ms += mm[k];
        }
        ms += __shfl_xor(ms, 16);
        ms += __shfl_xor(ms, 32);
        const float mean = ms * (1.0f / 16.0f);
#pragma unroll
        for (int k = 0; k < 4; ++k) { const float d = mm[k] - mean; m2 = fmaf(32.0f * d, d, m2); }
        m2 += __shfl_xor(m2, 16);
        m2 += __shfl_xor(m2, 32);
        if (q == 0) {
          LAS float* st = (LAS float*)(lds + L_ST5);
          st[16 * cw + fr] = mean;
          st[128 + 16 * cw + fr] = rsqrtf(m2 * (1.0f / DM) + EPS);
        }
      }
      LAS char* sl = lds + slot_of(u);
      s16x8 wa[2];
#pragma unroll
      for (int i = 0; i < 2; ++i) wa[i] = lds_tr2(sl + tr5[i][0], sl + tr5[i][1]);
#pragma unroll
      for (int m = 0; m < 4; ++m) {
        const s16x8 xb = lds_b128(sl + (4 * th + m) * 1024 + lane * 16);
#pragma unroll
        for (int i = 0; i < 2; ++i) acc[i][m] = mfma(wa[i], xb, acc[i][m]);
      }
#pragma unroll
      for (int i = 0; i < 2; ++i) cs[i] = mfma(wa[i], ones, cs[i]);
    }
    LAS float* st = (LAS float*)(lds + L_ST5);
#pragma unroll
    for (int m = 0; m < 4; ++m) {
      const float mean = st[64 * th + 16 * m + fr], rstd = st[128 + 64 * th + 16 * m + fr];
      u32x4 o;
#pragma unroll
      for (int i = 0; i < 2; ++i) {
        float g[4];
#pragma unroll
        for (int j = 0; j < 4; ++j) g[j] = gelu_tanh(rstd * fmaf(-mean, cs[i][j], acc[i][m][j]));
        o[2 * i] = pk_bf16(g[0], g[1]);
        o[2 * i + 1] = pk_bf16(g[2], g[3]);
      }
      __builtin_amdgcn_raw_buffer_store_b128(
          o, rs, WS_HID + ((2 * R5 + th) * 56 + 4 * c5 + n5) * 4096 + m * 1024 + lane * 16, 0, 16);
    }
    publish(lds, 4, ws, WS_F5 + (R5 * 16 + c5) * 8, lane);
    pos = 38;
  }

  f32x4 accS[2] = {}, accH[2] = {};
  {
    int co[2];
#pragma unroll
    for (int i = 0; i < 2; ++i) {
      const int row = 32 * np + 16 * i + fr;
      co[i] = 8192 + row * 128 + ((q ^ ((row >> 1) & 7)) * 16);
    }
#pragma unroll 1
    for (int u = 0; u < 16; ++u, ++pos) {
      bar();
      LAS char* sl = lds + slot_of(pos);
#pragma unroll
      for (int ks = 0; ks < 2; ++ks) {
        const s16x8 hb = lds_tr2(sl + ks * 4096 + trT[0], sl + ks * 4096 + trT[1]);
#pragma unroll
        for (int i = 0; i < 2; ++i) accS[i] = mfma(lds_b128(sl + (co[i] ^ (64 * ks))), hb, accS[i]);
      }
    }
#pragma unroll 1
    for (int u = 0; u < 28; ++u, ++pos) {
      bar();
      LAS char* sl = lds + slot_of(pos);
#pragma unroll
      for (int ks = 0; ks < 2; ++ks) {
        const s16x8 hb = lds_b128(sl + ks * 4096 + tt * 1024 + lane * 16);
#pragma unroll
        for (int i = 0; i < 2; ++i)
          accH[i] = mfma(lds_tr2(sl + 8192 + ks * 4096 + trP[i][0], sl + 8192 + ks * 4096 + trP[i][1]), hb, accH[i]);
      }
    }
  }
  {
    LAS char* stg = lds + L_STG + cw * 2048;
#pragma unroll
    for (int i = 0; i < 2; ++i) {
      const u32x2 dk = *(const u32x2*)(p.dskip + (col0 + 16 * i) * 2);
      const float xv[4] = {bf_lo(xq[i][0]), bf_hi(xq[i][0]), bf_lo(xq[i][1]), bf_hi(xq[i][1])};
      const float dv[4] = {bf_lo(dk[0]), bf_hi(dk[0]), bf_lo(dk[1]), bf_hi(dk[1])};
      const float x1v[4] = {bf_lo(x1p[2 * i]), bf_hi(x1p[2 * i]), bf_lo(x1p[2 * i + 1]), bf_hi(x1p[2 * i + 1])};
      f32x4 o;
#pragma unroll
      for (int j = 0; j < 4; ++j) {
        const float s = fmaf((xv[j] - mean_t) * rstd_t, dv[j], accS[i][j]);
        const float z = x1v[j] + accH[i][j];
        o[j] = fmaf(s, z * sigmoidf_(z), xv[j]);
      }
      *(LAS f32x4*)(stg + fr * 128 + (((4 * i + q) ^ (fr & 7)) * 16)) = o;
    }
#pragma unroll
    for (int ii = 0; ii < 2; ++ii) {
      const int row = 8 * ii + (lane >> 3), ck = lane & 7;
      const f32x4 v = *(LAS f32x4*)(stg + row * 128 + ((ck ^ (row & 7)) * 16));
      *(f32x4*)(p.out + (64 * r + 16 * tt + row) * DM + 64 * c + 32 * np + ck * 4) = v;
    }
  }
}

__global__ void __launch_bounds__(768, 3) fused_kernel(Params p) {
  extern __shared__ __attribute__((aligned(16))) char smem[];
  LAS char* lds = (LAS char*)smem;
  const int wave = __builtin_amdgcn_readfirstlane(threadIdx.x >> 6), lane = threadIdx.x & 63;
  const int g = blockIdx.x & 7, jx = blockIdx.x >> 3;
  const int r = (g >> 1) * 8 + (jx >> 2), c = (g & 1) * 4 + (jx & 3);
  const bool do5 = jx < 28;
  const int R5 = (g >> 1) * 4 + jx / 7, c5 = (g & 1) * 7 + jx % 7;
  if (wave >= 8) loader(p, lds, wave - 8, lane, r, c, R5, c5, do5);
  else compute(p, lds, wave, lane, r, c, R5, c5, do5);
}
}

extern "C" void kernel_launch(void* const* d_in, const int* in_sizes, int n_in, void* d_out, int out_size,
                              void* d_ws, size_t ws_size, hipStream_t stream) {
  (void)in_sizes; (void)n_in; (void)out_size; (void)ws_size;
  Params p;
  p.x = (const char*)d_in[0];
  p.lam_re = (const char*)d_in[1];
  p.lam_im = (const char*)d_in[2];
  p.b_re = (const char*)d_in[3];
  p.b_im = (const char*)d_in[4];
  p.c_re = (const char*)d_in[5];
  p.c_im = (const char*)d_in[6];
  p.log_dt = (const char*)d_in[7];
  p.dskip = (const char*)d_in[8];
  p.w1 = (const char*)d_in[10];
  p.w2 = (const char*)d_in[11];
  p.w3 = (const char*)d_in[12];
  p.w4 = (const char*)d_in[13];
  p.out = (float*)d_out;
  p.ws = (char*)d_ws;
  (void)hipFuncSetAttribute((const void*)fused_kernel, hipFuncAttributeMaxDynamicSharedMemorySize, LDS_BYTES);
  fused_kernel<<<256, 768, LDS_BYTES, stream>>>(p);
}
```

```cpp
#include <hip/hip_runtime.h>
#include <hip/hip_bf16.h>
#include <stdint.h>

typedef __attribute__((ext_vector_type(4))) short s16x4;
typedef __attribute__((ext_vector_type(8))) short s16x8;
typedef __attribute__((ext_vector_type(4))) float f32x4;
typedef __attribute__((ext_vector_type(2))) float f32x2;
typedef __attribute__((ext_vector_type(4))) unsigned u32x4;
typedef __attribute__((ext_vector_type(2))) unsigned u32x2;
typedef __attribute__((ext_vector_type(2))) __bf16 bf16x2v;
typedef unsigned long long u64;
#define LAS __attribute__((address_space(3)))
#define GAS __attribute__((address_space(1)))
#define DEVI __device__ __forceinline__

namespace {
constexpr int SEQ = 2048, DM = 512, FF = 1792;
constexpr float EPS = 1e-5f;
constexpr unsigned WS_EPOCH = 0, WS_TMO = 2048;
constexpr unsigned FS = 128;
constexpr unsigned WS_F1 = 65536, WS_F2 = WS_F1 + 256 * FS, WS_F3 = WS_F2 + 256 * FS, WS_F4 = WS_F3 + 256 * FS,
                   WS_F5 = WS_F4 + 256 * FS;
constexpr unsigned WS_H1S = 1u << 20;
constexpr unsigned WS_UT = WS_H1S + (2u << 20);
constexpr unsigned WS_X1 = WS_UT + (4u << 20);
constexpr unsigned WS_ST3 = WS_X1 + (2u << 20);
constexpr unsigned WS_HT = WS_ST3 + (1u << 20);
constexpr unsigned WS_HID = WS_HT + (4u << 20);
constexpr unsigned WS_END = WS_HID + (8u << 20);
constexpr int SLOT = 16384;
constexpr int L_XN = 4 * SLOT;
constexpr int L_STG = 8 * SLOT;
constexpr int L_MISC = L_STG + 8 * 2048;
constexpr int L_ST1 = L_MISC;
constexpr int L_ST5 = L_ST1 + 768;
constexpr int L_SCN = L_ST5 + 1024;
constexpr int L_CTL = L_SCN + 64;
constexpr int L_FULL = L_CTL;
constexpr int L_DONE = L_CTL + 32;
constexpr int L_CNT = L_CTL + 64;
constexpr int L_XF = L_CTL + 96;
constexpr int L_SF = L_CTL + 128;
constexpr int L_TAG = L_CTL + 256;
constexpr int LDS_BYTES = L_TAG + 32;
constexpr int NSTAGE = 81;
#define POLL_SLEEP 4
#define PUB1 2
#define PUB2 0
#define PUB5 4
#define J0_3 10
#define J1_3 12
#define J0_4 15
#define J1_4 18
#define J0_6 31
#define J1_6 34
#define J0_7 46
#define J1_7 49
constexpr unsigned LDS_SPIN_CAP = 1u << 19;
constexpr unsigned SPIN_CAP = 1u << 15;

struct Params {
  const char *x, *lam_re, *lam_im, *b_re, *b_im, *c_re, *c_im, *log_dt, *dskip, *w1, *w2, *w3, *w4;
  float* out;
  char* ws;
};

DEVI float as_f(unsigned u) { return __builtin_bit_cast(float, u); }
DEVI float bf_lo(unsigned u) { return __builtin_bit_cast(float, u << 16); }
DEVI float bf_hi(unsigned u) { return __builtin_bit_cast(float, u & 0xffff0000u); }
DEVI unsigned pk_bf16(float a, float b) {
  f32x2 p = {a, b};
  return __builtin_bit_cast(unsigned, __builtin_convertvector(p, bf16x2v));
}
DEVI float ld_bf16(const char* p, int i) {
  return __builtin_bit_cast(float, (unsigned)(*(const unsigned short*)(p + 2 * i)) << 16);
}
DEVI float rsq(float v) { return __builtin_amdgcn_rsqf(v); }
DEVI f32x2 rcp1p_exp2(f32x2 e) {
  const f32x2 d = f32x2{__builtin_amdgcn_exp2f(e[0]), __builtin_amdgcn_exp2f(e[1])} + 1.0f;
  return f32x2{__builtin_amdgcn_rcpf(d[0]), __builtin_amdgcn_rcpf(d[1])};
}
DEVI f32x2 gelu2(f32x2 v) {
  constexpr float c1 = -2.3022081983f, c2 = -0.1029432397f;
  return v * rcp1p_exp2(v * (v * v * c2 + c1));
}
DEVI f32x2 silu2(f32x2 z) { return z * rcp1p_exp2(z * -1.4426950409f); }
DEVI unsigned pk2(f32x2 v) { return __builtin_bit_cast(unsigned, __builtin_convertvector(v, bf16x2v)); }
DEVI f32x2 unpk(unsigned u) { return f32x2{bf_lo(u), bf_hi(u)}; }
DEVI f32x2 lo2(f32x4 v) { return f32x2{v[0], v[1]}; }
DEVI f32x2 hi2(f32x4 v) { return f32x2{v[2], v[3]}; }
template <class T>
DEVI void pin(T& v) { asm volatile("" : "+v"(v)); }
DEVI f32x4 mfma(s16x8 a, s16x8 b, f32x4 c) { return __builtin_amdgcn_mfma_f32_16x16x32_bf16(a, b, c, 0, 0, 0); }
DEVI s16x8 lds_b128(LAS char* p) { return *(LAS s16x8*)p; }
DEVI s16x8 lds_tr2(LAS char* p0, LAS char* p1) {
  s16x4 a = __builtin_amdgcn_ds_read_tr16_b64_v4i16((LAS s16x4*)p0);
  s16x4 b = __builtin_amdgcn_ds_read_tr16_b64_v4i16((LAS s16x4*)p1);
  return __builtin_shufflevector(a, b, 0, 1, 2, 3, 4, 5, 6, 7);
}
DEVI int hswz(int k) { return ((k >> 1) & 1) | ((((k >> 2) ^ (k >> 3)) & 1) << 1); }

template <int AUX>
DEVI void glds(const char* base, unsigned voff, LAS char* dst) {
  __builtin_amdgcn_global_load_lds((const GAS unsigned*)(base + voff), (LAS unsigned*)dst, 16, 0, AUX);
}
DEVI u64 ld_flag(const char* p) {
  return __hip_atomic_load((const GAS u64*)p, __ATOMIC_RELAXED, __HIP_MEMORY_SCOPE_AGENT);
}
DEVI void publish(LAS char* lds, int which, char* ws, unsigned flag_off, int lane) {
  asm volatile("s_waitcnt vmcnt(0)" ::: "memory");
  if (lane == 0) {
    const unsigned old = __hip_atomic_fetch_add((LAS unsigned*)(lds + L_CNT) + which, 1u, __ATOMIC_RELAXED,
                                                __HIP_MEMORY_SCOPE_WORKGROUP);
    if (old == 7u) {
      const u64 tag = *(volatile LAS u64*)(lds + L_TAG);
      __hip_atomic_store((GAS u64*)(ws + flag_off), tag, __ATOMIC_RELAXED, __HIP_MEMORY_SCOPE_AGENT);
    }
  }
}
#define STAMP(id) do {} while (0)
DEVI int slot_of(int u) { return (u < 12 ? (u & 3) : ((u - 12) & 7)) * SLOT; }

template <bool ISA, bool DO5>
struct Sched {
  static constexpr int skip = DO5 ? 0 : 16, npos = NSTAGE - skip;
  static constexpr int stage(int u) { return u < 21 ? u : u + skip; }
  static constexpr int type(int u) {
    const int s = stage(u);
    return s < 4 ? 1 : s < 12 ? 2 : s < 20 ? 3 : s < 21 ? 4 : s < 37 ? 5 : s < 53 ? 6 : 7;
  }
  static constexpr int first(int t) {
    return t == 1 ? 0 : t == 2 ? 4 : t == 3 ? 12 : t == 4 ? 20 : t == 5 ? 21 : t == 6 ? 37 - skip : t == 7 ? 53 - skip : npos;
  }
  static constexpr int cnt(int u) { return u == 20 ? (ISA ? 8 : 0) : 4; }
  static constexpr int prev(int u) { return u < 16 ? u - 4 : u < 20 ? 11 : u - 8; }
  static constexpr bool slot_free(int u, int j) {
    return prev(u) < 0 || j >= prev(u) + (type(prev(u)) == 5 ? 3 : 2);
  }
  static constexpr int j0(int t) {
    return t == 3 ? J0_3 : t == 4 ? J0_4 : (t == 6 && DO5) ? J0_6 : t == 7 ? J0_7 - skip : -1;
  }
  static constexpr int j1(int t) {
    return t == 3 ? J1_3 : t == 4 ? J1_4 : (t == 6 && DO5) ? J1_6 : t == 7 ? J1_7 - skip : first(t);
  }
  static constexpr int nl(int t) { return t == 6 ? 4 : 1; }
  static constexpr int lim(int j) {
    if (j < 0) return -1;
    int u = -1;
    while (u + 1 < npos && slot_free(u + 1, j) && !(ISA && type(u + 1) >= 3 && j < j1(type(u + 1)))) ++u;
    return u;
  }
  static constexpr int req_at(int j) {
    for (int t = 3; t <= 7; ++t) if (ISA && j0(t) == j) return t;
    return 0;
  }
  static constexpr int chk_at(int j) {
    for (int t = 3; t <= 7; ++t) if (ISA && j0(t) >= 0 && j1(t) == j) return t;
    return 0;
  }
  static constexpr int early(int j) {
    for (int t = 3; t <= 7; ++t) if (ISA && j0(t) >= 0 && j1(t) <= j && j < first(t)) return t;
    return 0;
  }
  static constexpr bool entry(int j) { return ISA && type(j) >= 3 && j == first(type(j)); }
  static constexpr int waitn(int j) {
    int n = 0;
    for (int u = j + 1; u <= lim(j); ++u) n += cnt(u);
    for (int t = 3; t <= 7; ++t) if (ISA && j0(t) >= 0 && j0(t) <= j && j < j1(t)) n += nl(t);
    return n;
  }
  static constexpr bool single(int j) {
    return lim(j) == lim(j - 1) + 1 && cnt(lim(j)) == 4 && !entry(j) && !req_at(j) && !chk_at(j) && !early(j);
  }
  static constexpr int runlen(int j) {
    if (!single(j)) return 1;
    int n = 1;
    while (j + n < npos && single(j + n) && type(lim(j + n)) == type(lim(j)) && waitn(j + n) == waitn(j)) ++n;
    return n;
  }
  static constexpr bool valid() {
    for (int t = 3; t <= 7; ++t) {
      if (t == 5 && !DO5) continue;
      if (j0(t) < 0) continue;
      if (!(j0(t) < j1(t) && j1(t) <= first(t))) return false;
      if (ISA && lim(j0(t)) != first(t) - 1) return false;
    }
    for (int j = 0; j < npos; ++j) if (lim(j) < j || waitn(j) > 63) return false;
    return true;
  }
};

struct LoadCtx {
  const Params* p;
  LAS char* lds;
  unsigned vK1[2], vK4[2], vM[2], vW3[2], vL;
  int lw, lane, r, c, R5, c5;
  bool ok;
  u64 tag, fl[4];
};

template <int N>
DEVI void wait_bar() {
  asm volatile("s_waitcnt vmcnt(%0)\n\ts_barrier" ::"i"(N) : "memory");
}

template <bool ISA, int T>
DEVI void issue(const LoadCtx& x, int u, int q) {
  const Params& p = *x.p;
  const int lw = x.lw, half = lw & 1, r = x.r, c = x.c;
  LAS char* dst = x.lds + slot_of(u) + lw * 4096;
  if constexpr (T == 1) {
    const char* b = p.w1 + (128 * q + 32 * lw) * 1024 + c * 128;
#pragma unroll
    for (int i = 0; i < 4; ++i) glds<0>(b + (i >> 1) * 16384, x.vK1[i & 1], dst + i * 1024);
  } else if constexpr (T == 2) {
    const char* b = (ISA ? p.b_re : p.b_im) + (64 * c + 32 * half) * 1024 + q * 128;
#pragma unroll
    for (int i = 0; i < 4; ++i) glds<0>(b + (i >> 1) * 16384, x.vM[i & 1], dst + i * 1024);
  } else if constexpr (T == 3) {
    if constexpr (ISA) {
      const char* b = p.ws + WS_H1S + (r * 16 + 2 * q + half) * 4096;
#pragma unroll
      for (int i = 0; i < 4; ++i) glds<16>(b + i * 1024, x.vL, dst + i * 1024);
    } else {
      const char* b = p.w2 + (64 * q + 32 * half) * 1024 + c * 128;
#pragma unroll
      for (int i = 0; i < 4; ++i) glds<0>(b + (i >> 1) * 16384, x.vK1[i & 1], dst + i * 1024);
    }
  } else if constexpr (T == 4) {
    const char* b = p.ws + WS_UT + (2 * (int)blockIdx.x + lw) * 4096;
    LAS char* d2 = x.lds + slot_of(u) + lw * 8192;
#pragma unroll
    for (int i = 0; i < 8; ++i) glds<16>(b + (i >> 2) * (512 * 4096) + (i & 3) * 1024, x.vL, d2 + i * 1024);
  } else if constexpr (T == 5) {
    if constexpr (ISA) {
      const char* b = p.ws + WS_X1 + (x.R5 * 16 + q) * 8192 + half * 4096;
#pragma unroll
      for (int i = 0; i < 4; ++i) glds<16>(b + i * 1024, x.vL, dst + i * 1024);
    } else {
      const char* b = p.w3 + (32 * q + 16 * half) * (FF * 2) + x.c5 * 256;
#pragma unroll
      for (int i = 0; i < 4; ++i) glds<0>(b + (i >> 1) * (8 * FF * 2), x.vW3[i & 1], dst + i * 1024);
    }
  } else if constexpr (T == 6) {
    if constexpr (ISA) {
      const char* b = p.ws + WS_HT + (64 * q + 32 * half) * 4096 + r * 128;
#pragma unroll
      for (int i = 0; i < 4; ++i) glds<16>(b + (i >> 1) * (16 * 4096), x.vK4[i & 1], dst + i * 1024);
    } else {
      const char* b = (q < 8 ? p.c_re : p.c_im) + (64 * c + 32 * half) * 1024 + (q & 7) * 128;
#pragma unroll
      for (int i = 0; i < 4; ++i) glds<0>(b + (i >> 1) * 16384, x.vM[i & 1], dst + i * 1024);
    }
  } else {
    if constexpr (ISA) {
      const char* b = p.ws + WS_HID + (r * 56 + 2 * q + half) * 4096;
#pragma unroll
      for (int i = 0; i < 4; ++i) glds<16>(b + i * 1024, x.vL, dst + i * 1024);
    } else {
      const char* b = p.w4 + (64 * q + 32 * half) * 1024 + c * 128;
#pragma unroll
      for (int i = 0; i < 4; ++i) glds<0>(b + (i >> 1) * 16384, x.vK1[i & 1], dst + i * 1024);
    }
  }
}

template <int T>
DEVI const char* flag_ptr(const LoadCtx& x) {
  const char* const ws = x.p->ws;
  const int lane = x.lane;
  if constexpr (T == 3) {
    const int r0 = x.r > 0 ? x.r - 1 : 0, r1 = x.r < 31 ? x.r + 1 : 31;
    return ws + WS_F1 + FS * (r0 * 8 + (lane < (r1 - r0 + 1) * 8 ? lane : 0));
  } else if constexpr (T == 4) {
    return ws + WS_F2 + FS * (((int)blockIdx.x >> 5) * 32 + (lane & 31));
  } else if constexpr (T == 5) {
    return ws + WS_F3 + FS * (x.R5 * 16 + (lane & 15));
  } else if constexpr (T == 6) {
    return ws + WS_F4 + FS * lane;
  } else {
    return ws + WS_F5 + FS * ((x.r >> 1) * 16 + (lane < 14 ? lane : 0));
  }
}
template <int T>
DEVI void flags_request(LoadCtx& x) {
  const char* fp = flag_ptr<T>(x);
#pragma unroll
  for (int i = 0; i < (T == 6 ? 4 : 1); ++i) x.fl[i] = ld_flag(fp + 64 * FS * i);
}
template <int T>
DEVI bool flags_match(const LoadCtx& x) {
  bool ok = true;
#pragma unroll
  for (int i = 0; i < (T == 6 ? 4 : 1); ++i) ok &= (x.fl[i] == x.tag);
  return __all(ok);
}
template <int T>
DEVI void flags_wait(LoadCtx& x) {
  char* const ws = x.p->ws;
#pragma unroll 1
  for (unsigned spins = 0;; ++spins) {
    flags_request<T>(x);
    if (flags_match<T>(x)) break;
    __builtin_amdgcn_s_sleep(POLL_SLEEP);
    if ((spins & 63u) == 63u && ld_flag(ws + WS_TMO) == x.tag) break;
    if (spins > SPIN_CAP) {
      __hip_atomic_store((GAS u64*)(ws + WS_TMO), x.tag, __ATOMIC_RELAXED, __HIP_MEMORY_SCOPE_AGENT);
      break;
    }
  }
}

template <bool ISA, bool DO5, int U, int UEND>
DEVI void issue_range(const LoadCtx& x) {
  using S = Sched<ISA, DO5>;
  if constexpr (U <= UEND) {
    constexpr int t = S::type(U), q = U - S::first(t);
    if constexpr (S::cnt(U) > 0) issue<ISA, t>(x, U, q);
    issue_range<ISA, DO5, U + 1, UEND>(x);
  }
}

template <bool ISA, bool DO5, int J>
DEVI void run_from(LoadCtx& x) {
  using S = Sched<ISA, DO5>;
  static_assert(S::valid(), "loader schedule");
  if constexpr (J < S::npos) {
    constexpr int n = S::runlen(J);
    if constexpr (n > 1) {
      constexpr int u0 = S::lim(J), t = S::type(u0), f0 = S::first(t);
#pragma unroll 1
      for (int u = u0; u < u0 + n; ++u) {
        issue<ISA, t>(x, u, u - f0);
        wait_bar<S::waitn(J)>();
      }
    } else {
      [[maybe_unused]] const Params& p = *x.p;
      [[maybe_unused]] const int lane = x.lane;
      if constexpr (J == 1 && ISA) {
        u32x2 tg;
        asm volatile("ds_read_b64 %0, %1\n\ts_waitcnt lgkmcnt(0)" : "=v"(tg) : "v"((unsigned)(size_t)(x.lds + L_TAG)) : "memory");
        const unsigned lo = tg[0], hi = tg[1];
        x.tag = ((u64)hi << 32) | lo;
      }
      if constexpr (S::chk_at(J) != 0) {
        x.ok = flags_match<S::chk_at(J)>(x);
        asm volatile("s_waitcnt vmcnt(0)" ::: "memory");
      }
      if constexpr (S::entry(J)) {
        constexpr int t = S::type(J);
        if (S::j0(t) < 0 || !x.ok) {
          STAMP(2 * t);
          flags_wait<t>(x);
          STAMP(2 * t + 1);
          asm volatile("s_waitcnt vmcnt(0)" ::: "memory");
          issue_range<ISA, DO5, J, S::lim(J - 1)>(x);
        }
        if constexpr (t == 7) {
          if (blockIdx.x == 0 && x.lw == 0 && lane == 0)
            __hip_atomic_store((GAS u64*)(p.ws + WS_EPOCH), x.tag, __ATOMIC_RELAXED, __HIP_MEMORY_SCOPE_AGENT);
        }
      }
      if constexpr (S::early(J) != 0) {
        if (x.ok) issue_range<ISA, DO5, S::lim(J - 1) + 1, S::lim(J)>(x);
      } else {
        issue_range<ISA, DO5, S::lim(J - 1) + 1, S::lim(J)>(x);
      }
      if constexpr (S::req_at(J) != 0) flags_request<S::req_at(J)>(x);
      wait_bar<S::waitn(J)>();
    }
    run_from<ISA, DO5, J + n>(x);
  }
}

DEVI void loader(const Params& p, LAS char* lds, int lw, int lane, int r, int c, int R5, int c5, bool do5) {
  LoadCtx x;
  x.p = &p; x.lds = lds; x.lw = lw; x.lane = lane; x.r = r; x.c = c; x.R5 = R5; x.c5 = c5; x.tag = 0; x.ok = false;
#pragma unroll
  for (int e = 0; e < 2; ++e) {
    const int row = 8 * e + (lane >> 3), ch = lane & 7;
    const int sk = ((((ch >> 1) ^ hswz(row)) << 1) | (ch & 1)) * 16;
    x.vK1[e] = row * 1024 + sk;
    x.vK4[e] = row * 4096 + sk;
    x.vM[e] = row * 1024 + ((ch ^ ((row >> 1) & 7)) * 16);
    const int row3 = 4 * e + (lane >> 4), ch3 = lane & 15;
    x.vW3[e] = row3 * (FF * 2) + (((((ch3 >> 1) ^ (row3 & 7)) << 1) | (ch3 & 1)) * 16);
  }
  x.vL = lane * 16;
  if (lw < 2) {
    if (do5) run_from<true, true, 0>(x);
    else run_from<true, false, 0>(x);
  } else {
    if (do5) run_from<false, true, 0>(x);
    else run_from<false, false, 0>(x);
  }
}

DEVI unsigned as_u(float f) { return __builtin_bit_cast(unsigned, f); }
DEVI float fold32(float a, float b) {
  const auto r = __builtin_amdgcn_permlane32_swap(as_u(a), as_u(b), false, false);
  const unsigned r0 = r[0], r1 = r[1];
  return as_f(r0) + as_f(r1);
}
DEVI float fold16(float a, float b) {
  const auto r = __builtin_amdgcn_permlane16_swap(as_u(a), as_u(b), false, false);
  const unsigned r0 = r[0], r1 = r[1];
  return as_f(r0) + as_f(r1);
}
template <int CTRL, int ROWMASK = 0xf>
DEVI float dpp0(float v) {
  return as_f(__builtin_amdgcn_update_dpp(0u, as_u(v), CTRL, ROWMASK, 0xf, true));
}
DEVI float row_sum(float v) {
  v += dpp0<0x128>(v);
  v += dpp0<0x124>(v);
  v += dpp0<0x122>(v);
  v += dpp0<0x121>(v);
  return v;
}
DEVI float quad_col_sum(float v) {
  v = fold32(v, v);
  return fold16(v, v);
}
template <int CTRL, int ROWMASK = 0xf>
DEVI f32x2 dpp0c(f32x2 v) { return f32x2{dpp0<CTRL, ROWMASK>(v[0]), dpp0<CTRL, ROWMASK>(v[1])}; }
DEVI f32x2 cmul(f32x2 a, f32x2 b) {
  f32x2 r = {a[0] * b[0] - a[1] * b[1], a[0] * b[1] + a[1] * b[0]};
  return r;
}
DEVI f32x2 cfma(f32x2 a, f32x2 b, f32x2 c) {
  f32x2 r = {fmaf(a[0], b[0], fmaf(-a[1], b[1], c[0])), fmaf(a[0], b[1], fmaf(a[1], b[0], c[1]))};
  return r;
}

struct Cons {
  LAS char* lds;
  int cw, lane;
};
template <int NA, int NB>
struct Frag { s16x8 a[NA], b[NB]; };
template <bool DRAIN>
DEVI void cbar() {
  __builtin_amdgcn_sched_barrier(0);
  if constexpr (DRAIN) asm volatile("s_waitcnt lgkmcnt(0)\n\ts_barrier" ::: "memory");
  else asm volatile("s_barrier" ::: "memory");
  __builtin_amdgcn_sched_barrier(0);
}
template <int NR, int NM>
DEVI void interleave() {
  constexpr int per = (NR + NM - 1) / NM;
  __builtin_amdgcn_sched_group_barrier(0x2, 8, 0);
#pragma unroll
  for (int i = 0; i < NM; ++i) {
    if (i * per < NR) __builtin_amdgcn_sched_group_barrier(0x100, per, 0);
    __builtin_amdgcn_sched_group_barrier(0x8, 1, 0);
  }
}
template <int N, bool DRAIN, int MID, int NR, int NM, class F, class LD, class MM, class PR, class MD>
DEVI void pipe(int pos0, LD ld, MM mm, PR pre, MD mid) {
  F f0, f1;
  cbar<true>();
  ld(f0, pos0);
  pre();
#pragma unroll 1
  for (int u = pos0 + 1; u < pos0 + N - 1; u += 2) {
    cbar<DRAIN>();
    ld(f1, u);
    mm(f0);
    interleave<NR, NM>();
    cbar<DRAIN>();
    ld(f0, u + 1);
    mm(f1);
    interleave<NR, NM>();
    __builtin_amdgcn_sched_barrier(0);
    if (MID > 0 && u + 1 == pos0 + MID) mid();
  }
  cbar<DRAIN>();
  ld(f1, pos0 + N - 1);
  mm(f0);
  interleave<NR, NM>();
  __builtin_amdgcn_sched_barrier(0);
  mm(f1);
}
template <int A, int B>
DEVI void zero_pin(f32x4 (&acc)[A][B]) {
#pragma unroll
  for (int i = 0; i < A; ++i)
#pragma unroll
    for (int j = 0; j < B; ++j) { acc[i][j] = f32x4{0.f, 0.f, 0.f, 0.f}; pin(acc[i][j]); }
}
DEVI void xch_send(const Cons& k, const f32x4 (&acc)[2][2], int kh, int seq) {
  LAS char* stg = k.lds + L_STG + k.cw * 2048 + k.lane * 16;
  *(LAS f32x4*)stg = kh ? acc[0][0] : acc[0][1];
  *(LAS f32x4*)(stg + 1024) = kh ? acc[1][0] : acc[1][1];
  asm volatile("" ::: "memory");
  *(volatile LAS int*)(k.lds + L_XF + 4 * k.cw) = seq;
}
DEVI void xch_recv(const Cons& k, const f32x4 (&acc)[2][2], int kh, int seq, f32x4 (&out)[2]) {
#pragma unroll 1
  for (unsigned spins = 0; spins < LDS_SPIN_CAP; ++spins)
    if (__builtin_amdgcn_readfirstlane(*(volatile LAS int*)(k.lds + L_XF + 4 * (k.cw ^ 4))) >= seq) break;
  asm volatile("" ::: "memory");
  LAS char* stg = k.lds + L_STG + (k.cw ^ 4) * 2048 + k.lane * 16;
  out[0] = (kh ? acc[0][1] : acc[0][0]) + *(LAS f32x4*)stg;
  out[1] = (kh ? acc[1][1] : acc[1][0]) + *(LAS f32x4*)(stg + 1024);
}

DEVI void compute(const Params& p, LAS char* lds, int cw, int lane, int r, int c, int R5, int c5, bool do5) {
  const int fr = lane & 15, q = lane >> 4;
  char* const ws = p.ws;
  const auto rs = __builtin_amdgcn_make_buffer_rsrc(ws, 0, (int)WS_END, 0x00020000);
  const int kh = cw >> 2, np = cw & 1, t2 = (cw >> 1) & 1, tt = 2 * t2 + kh;
  const int tok = 64 * r + 16 * tt + fr;
  const int col0 = 64 * c + 32 * np + 4 * q;
  Cons k;
  k.lds = lds; k.cw = cw; k.lane = lane;
  auto nop = [] {};

  STAMP(0);
  u32x4 xr[8];
#pragma unroll
  for (int i = 0; i < 8; ++i) xr[i] = *(const u32x4*)(p.x + (64 * r + 8 * cw + i) * 1024 + lane * 16);
  u32x2 xq[2];
#pragma unroll
  for (int i = 0; i < 2; ++i) xq[i] = *(const u32x2*)(p.x + tok * 1024 + (col0 + 16 * i) * 2);
  u64 epoch = 0;
  if (cw == 0) epoch = ld_flag(ws + WS_EPOCH);
  if (cw == 1) *((LAS unsigned*)(lds + L_CTL) + lane) = 0u;
  const int chl = cw >> 2, wv = cw & 3, ch = 2 * (int)blockIdx.x + chl;
  f32x2 a, coef, pw[7], pl, m16, m32;
  {
    const float lre = ld_bf16(p.lam_re, ch), lim = ld_bf16(p.lam_im, ch);
    const float dt = bf_lo(pk_bf16(expf(ld_bf16(p.log_dt, ch)), 0.f));
    float sn, cs;
    sincosf(dt * lim, &sn, &cs);
    const float mag = expf(dt * lre);
    a = f32x2{mag * cs, mag * sn};
    const float den = 1.0f / (lre * lre + lim * lim);
    const f32x2 am1 = {a[0] - 1.0f, a[1]}, linv = {lre * den, -lim * den};
    coef = cmul(am1, linv) * 2.0f;
    f32x2 t = cmul(a, a);
    t = cmul(t, t);
    pw[0] = cmul(t, t);
#pragma unroll
    for (int i = 1; i < 7; ++i) pw[i] = cmul(pw[i - 1], pw[i - 1]);
    pl = f32x2{1.0f, 0.0f};
#pragma unroll
    for (int i = 0; i < 6; ++i) {
      if (i == 4) m16 = cmul(pl, pw[0]);
      if (i == 5) m32 = cmul(pl, pw[0]);
      const f32x2 m = cmul(pl, pw[i]);
      if ((lane >> i) & 1) pl = m;
    }
    pin(pl); pin(m16); pin(m32);
    auto uni = [](f32x2& v) {
      v = f32x2{as_f(__builtin_amdgcn_readfirstlane(as_u(v[0]))), as_f(__builtin_amdgcn_readfirstlane(as_u(v[1])))};
    };
    uni(a); uni(coef); uni(pw[0]); uni(pw[1]); uni(pw[2]); uni(pw[3]); uni(pw[6]);
  }
  {
    float sm[8], sq[8];
#pragma unroll
    for (int i = 0; i < 8; ++i) {
      f32x2 s2 = {0.f, 0.f}, q2 = {0.f, 0.f};
#pragma unroll
      for (int j = 0; j < 4; ++j) {
        const f32x2 v = {bf_lo(xr[i][j]), bf_hi(xr[i][j])};
        s2 += v;
        q2 += v * v;
      }
      sm[i] = s2[0] + s2[1];
      sq[i] = q2[0] + q2[1];
    }
    float mean2[2], rstd2[2];
#pragma unroll
    for (int e = 0; e < 2; ++e) {
      const float s = row_sum(fold16(fold32(sm[e], sm[4 + e]), fold32(sm[2 + e], sm[6 + e])));
      const float qq = row_sum(fold16(fold32(sq[e], sq[4 + e]), fold32(sq[2 + e], sq[6 + e])));
      const float mean = s * (1.0f / DM);
      const float var = fmaxf(fmaf(-mean, mean, qq * (1.0f / DM)), 0.f);
      const float rstd = rsq(var + EPS);
      mean2[e] = mean;
      rstd2[e] = rstd;
      LAS float* st = (LAS float*)(lds + L_ST1) + 8 * cw + 2 * q + e;
      st[0] = mean;
      st[64] = rstd;
      st[128] = rsq(var * rstd * rstd + EPS);
    }
#pragma unroll
    for (int i = 0; i < 8; ++i) {
      const float rs_ = as_f(__builtin_amdgcn_readlane(as_u(rstd2[i & 1]), 16 * (i >> 1)));
      const float mr = -as_f(__builtin_amdgcn_readlane(as_u(mean2[i & 1]), 16 * (i >> 1))) * rs_;
      u32x4 o;
#pragma unroll
      for (int j = 0; j < 4; ++j) o[j] = pk_bf16(fmaf(bf_lo(xr[i][j]), rs_, mr), fmaf(bf_hi(xr[i][j]), rs_, mr));
      const int row = 8 * cw + i;
      *(LAS u32x4*)(lds + L_XN + row * 1024 + ((lane ^ (row & 15)) * 16)) = o;
    }
  }
  if (cw == 0) *(LAS u64*)(lds + L_TAG) = epoch + 1ull;

  const int tq = fr >> 2, tp = fr & 3;
  int trN[2][2], trP[2][2], trT[2][2];
#pragma unroll
  for (int t = 0; t < 2; ++t) {
    const int rn = 8 * q + 4 * t + tq, rp = 16 * t + 4 * q + tq;
#pragma unroll
    for (int i = 0; i < 2; ++i) {
      trN[i][t] = kh * 4096 + rn * 128 + (((2 * np + i) ^ hswz(rn)) * 32) + tp * 8;
      trP[i][t] = 8192 + kh * 4096 + rp * 128 + (((2 * np + i) ^ hswz(rp)) * 32) + tp * 8;
      trT[i][t] = kh * 4096 + rn * 128 + (((2 * t2 + i) ^ hswz(rn)) * 32) + tp * 8;
    }
  }
  const int linT = kh * 4096 + 2 * t2 * 1024 + lane * 16;
  const int xsw = (q ^ fr) * 16;
  STAMP(1);
  float mean_t, rstd_t, r2_t;

  {
    f32x4 acc[2][2];
    zero_pin(acc);
    using F = Frag<4, 4>;
    pipe<4, true, 0, 12, 8, F>(0,
      [&](F& f, int u) {
        LAS char* sl = lds + slot_of(u);
        LAS char* xa = lds + L_XN + (32 * t2 + fr) * 1024 + u * 256;
#pragma unroll
        for (int h = 0; h < 2; ++h)
#pragma unroll
          for (int i = 0; i < 2; ++i) {
            f.a[2 * h + i] = lds_tr2(sl + h * 8192 + trN[i][0], sl + h * 8192 + trN[i][1]);
            f.b[2 * h + i] = lds_b128(xa + i * 16384 + ((128 * h + 64 * kh) ^ xsw));
          }
      },
      [&](const F& f) {
#pragma unroll
        for (int h = 0; h < 2; ++h)
#pragma unroll
          for (int i = 0; i < 2; ++i)
#pragma unroll
            for (int m = 0; m < 2; ++m) acc[i][m] = mfma(f.a[2 * h + i], f.b[2 * h + m], acc[i][m]);
      },
      [&] {
        mean_t = ((LAS float*)(lds + L_ST1))[16 * tt + fr];
        rstd_t = ((LAS float*)(lds + L_ST1))[64 + 16 * tt + fr];
        r2_t = ((LAS float*)(lds + L_ST1))[128 + 16 * tt + fr];
      },
      nop);
    STAMP(2);
    f32x4 res[2];
    xch_send(k, acc, kh, 1);
    xch_recv(k, acc, kh, 1, res);
    u32x4 o;
#pragma unroll
    for (int i = 0; i < 2; ++i) {
      o[2 * i] = pk2(gelu2(lo2(res[i]) * r2_t));
      o[2 * i + 1] = pk2(gelu2(hi2(res[i]) * r2_t));
    }
    const int grp = c >> 1, dl = (grp & 1) ? -(1 + (grp >> 1)) : (1 + (grp >> 1));
    const int ks32 = 2 * c + np;
    const int td = tok + dl;
    if (td >= 0 && td < SEQ)
      __builtin_amdgcn_raw_buffer_store_b128(
          o, rs, WS_H1S + ((td >> 6) * 16 + ks32) * 4096 + ((td >> 4) & 3) * 1024 + (q * 16 + (td & 15)) * 16, 0, 16);
    if (tok - dl < 0 || tok - dl >= SEQ) {
      const u32x4 z = {0u, 0u, 0u, 0u};
      __builtin_amdgcn_raw_buffer_store_b128(
          z, rs, WS_H1S + ((tok >> 6) * 16 + ks32) * 4096 + ((tok >> 4) & 3) * 1024 + (q * 16 + (tok & 15)) * 16, 0, 16);
    }
  }

  STAMP(3);
  {
    const int th = cw & 1, pq = cw >> 1;
    f32x4 acc[2][2];
    zero_pin(acc);
    int bo[2];
#pragma unroll
    for (int nt = 0; nt < 2; ++nt) {
      const int row = 32 * pq + 16 * nt + fr;
      bo[nt] = row * 128 + ((q ^ ((row >> 1) & 7)) * 16);
    }
    using F = Frag<4, 4>;
    pipe<8, true, PUB1, 8, 8, F>(4,
      [&](F& f, int u) {
        LAS char* sl = lds + slot_of(u);
        const int s2 = u - 4;
        LAS char* xa = lds + L_XN + (32 * th + fr) * 1024 + (s2 >> 1) * 256;
#pragma unroll
        for (int ks = 0; ks < 2; ++ks)
#pragma unroll
          for (int i = 0; i < 2; ++i) {
            f.a[2 * ks + i] = lds_b128(xa + i * 16384 + ((128 * (s2 & 1) + 64 * ks) ^ xsw));
            f.b[2 * ks + i] = lds_b128(sl + (bo[i] ^ (64 * ks)));
          }
      },
      [&](const F& f) {
#pragma unroll
        for (int ks = 0; ks < 2; ++ks)
#pragma unroll
          for (int mt = 0; mt < 2; ++mt)
#pragma unroll
            for (int nt = 0; nt < 2; ++nt) acc[mt][nt] = mfma(f.a[2 * ks + mt], f.b[2 * ks + nt], acc[mt][nt]);
      },
      nop, [&] { publish(lds, 0, ws, WS_F1 + (r * 8 + c) * FS, lane); });
    STAMP(4);
    LAS char* stg = lds + L_STG + cw * 2048;
#pragma unroll
    for (int nt = 0; nt < 2; ++nt)
#pragma unroll
      for (int mt = 0; mt < 2; ++mt) {
        const int row = 16 * nt + fr;
        const u32x2 v = {pk_bf16(acc[mt][nt][0], acc[mt][nt][1]), pk_bf16(acc[mt][nt][2], acc[mt][nt][3])};
        *(LAS u32x2*)(stg + row * 64 + (((4 * mt + q) ^ (((row >> 1) & 3) << 1)) * 8)) = v;
      }
#pragma unroll
    for (int ii = 0; ii < 2; ++ii) {
      const int row = 16 * ii + (lane >> 2), pr = lane & 3;
      const u32x4 v = *(LAS u32x4*)(stg + row * 64 + ((pr ^ ((row >> 1) & 3)) * 16));
      const int prow = (pq >> 1) * 512 + 64 * c + 32 * (pq & 1) + row;
      __builtin_amdgcn_raw_buffer_store_b128(v, rs, WS_UT + prow * 4096 + (64 * r + 32 * th) * 2 + pr * 16, 0, 16);
    }
    if (PUB2 == 0) publish(lds, 1, ws, WS_F2 + (c * 32 + r) * FS, lane);
  }

  using F22 = Frag<2, 2>;
  auto ld_wt = [&](F22& f, int u) {
    LAS char* sl = lds + slot_of(u);
#pragma unroll
    for (int i = 0; i < 2; ++i) {
      f.a[i] = lds_tr2(sl + trP[i][0], sl + trP[i][1]);
      f.b[i] = lds_b128(sl + linT + i * 1024);
    }
  };

  STAMP(5);
  u32x4 x1p;
  {
    f32x4 acc[2][2];
    zero_pin(acc);
    pipe<8, true, PUB2, 6, 4, F22>(12, ld_wt, [&](const F22& f) {
#pragma unroll
      for (int i = 0; i < 2; ++i)
#pragma unroll
        for (int m = 0; m < 2; ++m) acc[i][m] = mfma(f.a[i], f.b[m], acc[i][m]);
    }, nop, [&] { publish(lds, 1, ws, WS_F2 + (c * 32 + r) * FS, lane); });
    STAMP(6);
    f32x4 res[2];
    xch_send(k, acc, kh, 2);
    xch_recv(k, acc, kh, 2, res);
#pragma unroll
    for (int i = 0; i < 2; ++i) {
      x1p[2 * i] = pk2((unpk(xq[i][0]) - mean_t) * rstd_t + lo2(res[i]));
      x1p[2 * i + 1] = pk2((unpk(xq[i][1]) - mean_t) * rstd_t + hi2(res[i]));
    }
    const int ks32 = 2 * c + np;
    __builtin_amdgcn_raw_buffer_store_b128(
        x1p, rs, WS_X1 + ((tok >> 7) * 16 + ks32) * 8192 + ((tok >> 4) & 7) * 1024 + lane * 16, 0, 16);
    float v[8];
#pragma unroll
    for (int j = 0; j < 4; ++j) { v[2 * j] = bf_lo(x1p[j]); v[2 * j + 1] = bf_hi(x1p[j]); }
    const float s = quad_col_sum(((v[0] + v[1]) + (v[2] + v[3])) + ((v[4] + v[5]) + (v[6] + v[7])));
    const float m32 = s * (1.0f / 32.0f);
    float m2 = 0.f;
#pragma unroll
    for (int j = 0; j < 8; ++j) { const float d = v[j] - m32; m2 = fmaf(d, d, m2); }
    m2 = quad_col_sum(m2);
    if (q == 0) {
      const u32x2 sv = {as_u(m32), as_u(m2)};
      __builtin_amdgcn_raw_buffer_store_b64(sv, rs, WS_ST3 + (ks32 * SEQ + tok) * 8, 0, 16);
    }
  }

  STAMP(7);
  {
    STAMP(8);
    cbar<true>();
    STAMP(9);
    LAS char* sl = lds + slot_of(20) + chl * 8192 + wv * 1024 + lane * 16;
    const u32x4 ur = *(LAS u32x4*)sl, ui = *(LAS u32x4*)(sl + 4096);
    f32x2 v[8];
#pragma unroll
    for (int j = 0; j < 4; ++j) {
      v[2 * j] = f32x2{bf_lo(ur[j]), bf_lo(ui[j])};
      v[2 * j + 1] = f32x2{bf_hi(ur[j]), bf_hi(ui[j])};
    }
    f32x2 T = v[0];
#pragma unroll
    for (int j = 1; j < 8; ++j) T = cfma(a, T, v[j]);
    T = cfma(pw[0], dpp0c<0x111>(T), T);
    T = cfma(pw[1], dpp0c<0x112>(T), T);
    T = cfma(pw[2], dpp0c<0x114>(T), T);
    T = cfma(pw[3], dpp0c<0x118>(T), T);
    T = cfma(m16, dpp0c<0x142, 0xa>(T), T);
    T = cfma(m32, dpp0c<0x143, 0xc>(T), T);
    if (lane == 63) {
      *(LAS f32x2*)(lds + L_SCN + cw * 8) = T;
      asm volatile("" ::: "memory");
      *(volatile LAS int*)(lds + L_SF + 4 * cw) = 1;
    }
    f32x2 P = dpp0c<0x138>(T);
    STAMP(16);
    publish(lds, 2, ws, WS_F3 + (r * 8 + c) * FS, lane);
    STAMP(17);
#pragma unroll 1
    for (unsigned spins = 0; spins < LDS_SPIN_CAP; ++spins) {
      const int fl = *(volatile LAS int*)(lds + L_SF + 4 * (4 * chl + (lane & 3)));
      if (__all(fl != 0 || (lane & 3) >= wv)) break;
    }
    asm volatile("" ::: "memory");
    STAMP(18);
    f32x2 cin = {0.f, 0.f};
#pragma unroll
    for (int w = 0; w < 3; ++w) {
      if (w < wv) {
        const f32x2 tw = *(LAS f32x2*)(lds + L_SCN + (4 * chl + w) * 8);
        cin = cfma(pw[6], cin, tw);
      }
    }
    P = cfma(pl, cin, P);
    float hr[8], hi[8];
#pragma unroll
    for (int j = 0; j < 8; ++j) {
      P = cfma(a, P, v[j]);
      const f32x2 h = cmul(coef, P);
      hr[j] = h[0];
      hi[j] = -h[1];
    }
    u32x4 orr, oi;
#pragma unroll
    for (int j = 0; j < 4; ++j) { orr[j] = pk_bf16(hr[2 * j], hr[2 * j + 1]); oi[j] = pk_bf16(hi[2 * j], hi[2 * j + 1]); }
    __builtin_amdgcn_raw_buffer_store_b128(orr, rs, WS_HT + ch * 4096 + wv * 1024 + lane * 16, 0, 16);
    __builtin_amdgcn_raw_buffer_store_b128(oi, rs, WS_HT + (512 + ch) * 4096 + wv * 1024 + lane * 16, 0, 16);
    STAMP(19);
    publish(lds, 3, ws, WS_F4 + (int)blockIdx.x * FS, lane);
  }

  STAMP(10);
  int pos = 21;
  if (do5) {
    const int n5 = cw & 3, th = cw >> 2;
    int tr5[2][2];
#pragma unroll
    for (int t = 0; t < 2; ++t) {
      const int rp = 16 * t + 4 * q + tq;
#pragma unroll
      for (int i = 0; i < 2; ++i) tr5[i][t] = 8192 + rp * 256 + (((2 * n5 + i) ^ (rp & 7)) * 32) + tp * 8;
    }
    u32x2 sv[4];
    f32x4 acc[2][4], csa[1][2];
    zero_pin(acc);
    zero_pin(csa);
    f32x4(&cs)[2] = csa[0];
#pragma unroll
    for (int t = 0; t < 2; ++t) { pin(tr5[0][t]); pin(tr5[1][t]); }
    unsigned so = WS_ST3 + (4 * q * SEQ + 128 * R5 + 16 * cw + fr) * 8;
    pin(so);
    const s16x8 ones = {0x3f80, 0x3f80, 0x3f80, 0x3f80, 0x3f80, 0x3f80, 0x3f80, 0x3f80};
    using F = Frag<2, 4>;
    auto ld5 = [&](F& f, int u) {
      LAS char* sl = lds + slot_of(u);
#pragma unroll
      for (int i = 0; i < 2; ++i) f.a[i] = lds_tr2(sl + tr5[i][0], sl + tr5[i][1]);
#pragma unroll
      for (int m = 0; m < 4; ++m) f.b[m] = lds_b128(sl + (4 * th + m) * 1024 + lane * 16);
    };
    auto mm5 = [&](const F& f) {
#pragma unroll
      for (int m = 0; m < 4; ++m)
#pragma unroll
        for (int i = 0; i < 2; ++i) acc[i][m] = mfma(f.a[i], f.b[m], acc[i][m]);
#pragma unroll
      for (int i = 0; i < 2; ++i) cs[i] = mfma(f.a[i], ones, cs[i]);
    };
    pipe<16, false, 4, 8, 10, F>(21, ld5, mm5, [&] {
#pragma unroll
      for (int j = 0; j < 4; ++j)
        sv[j] = __builtin_amdgcn_raw_buffer_load_b64(rs, so, j * SEQ * 8, 16);
    }, [&] {
      float mm[4], m2 = 0.f, ms = 0.f;
#pragma unroll
      for (int j = 0; j < 4; ++j) {
        pin(sv[j]);
        mm[j] = as_f(sv[j][0]);
        m2 += as_f(sv[j][1]);
        ms += mm[j];
      }
      const float mean = quad_col_sum(ms) * (1.0f / 16.0f);
#pragma unroll
      for (int j = 0; j < 4; ++j) { const float d = mm[j] - mean; m2 = fmaf(32.0f * d, d, m2); }
      m2 = quad_col_sum(m2);
      LAS float* st = (LAS float*)(lds + L_ST5);
      st[16 * cw + fr] = mean;
      st[128 + 16 * cw + fr] = rsq(m2 * (1.0f / DM) + EPS);
    });
    STAMP(11);
    LAS float* st = (LAS float*)(lds + L_ST5);
#pragma unroll
    for (int m = 0; m < 4; ++m) {
      const float mean = st[64 * th + 16 * m + fr], rstd = st[128 + 64 * th + 16 * m + fr];
      u32x4 o;
#pragma unroll
      for (int i = 0; i < 2; ++i) {
        o[2 * i] = pk2(gelu2((lo2(acc[i][m]) - lo2(cs[i]) * mean) * rstd));
        o[2 * i + 1] = pk2(gelu2((hi2(acc[i][m]) - hi2(cs[i]) * mean) * rstd));
      }
      __builtin_amdgcn_raw_buffer_store_b128(
          o, rs, WS_HID + ((2 * R5 + th) * 56 + 4 * c5 + n5) * 4096 + m * 1024 + lane * 16, 0, 16);
    }
    pos = 37;
  }

  STAMP(12);
  f32x4 resS[2], resH[2];
  u32x2 dk[2];
#pragma unroll
  for (int i = 0; i < 2; ++i) dk[i] = *(const u32x2*)(p.dskip + (col0 + 16 * i) * 2);
  {
    f32x4 accS[2][2], accH[2][2];
    zero_pin(accS);
    zero_pin(accH);
    int co[2];
#pragma unroll
    for (int i = 0; i < 2; ++i) {
      const int row = 32 * np + 16 * i + fr;
      co[i] = 8192 + row * 128 + (((4 * kh + q) ^ ((row >> 1) & 7)) * 16);
    }
    pipe<16, true, PUB5, 6, 4, F22>(pos,
      [&](F22& f, int u) {
        LAS char* sl = lds + slot_of(u);
#pragma unroll
        for (int i = 0; i < 2; ++i) {
          f.a[i] = lds_b128(sl + co[i]);
          f.b[i] = lds_tr2(sl + trT[i][0], sl + trT[i][1]);
        }
      },
      [&](const F22& f) {
#pragma unroll
        for (int i = 0; i < 2; ++i)
#pragma unroll
          for (int m = 0; m < 2; ++m) accS[i][m] = mfma(f.a[i], f.b[m], accS[i][m]);
      },
      nop, [&] { if (do5) publish(lds, 4, ws, WS_F5 + (R5 * 16 + c5) * FS, lane); });
    STAMP(13);
    xch_send(k, accS, kh, 3);
    auto mmH = [&](const F22& f) {
#pragma unroll
      for (int i = 0; i < 2; ++i)
#pragma unroll
        for (int m = 0; m < 2; ++m) accH[i][m] = mfma(f.a[i], f.b[m], accH[i][m]);
    };
    pipe<28, true, 2, 6, 4, F22>(pos + 16, ld_wt, mmH, nop, [&] { xch_recv(k, accS, kh, 3, resS); });
    STAMP(14);
    xch_send(k, accH, kh, 4);
    xch_recv(k, accH, kh, 4, resH);
  }
  {
    LAS char* stg = lds + L_STG + (cw ^ 4) * 2048;
#pragma unroll
    for (int i = 0; i < 2; ++i) {
      f32x2 oo[2];
#pragma unroll
      for (int h = 0; h < 2; ++h) {
        const f32x2 xv = unpk(xq[i][h]);
        const f32x2 sv = (xv - mean_t) * rstd_t * unpk(dk[i][h]) + (h ? hi2(resS[i]) : lo2(resS[i]));
        const f32x2 z = unpk(x1p[2 * i + h]) + (h ? hi2(resH[i]) : lo2(resH[i]));
        oo[h] = sv * silu2(z) + xv;
      }
      const f32x4 o = {oo[0][0], oo[0][1], oo[1][0], oo[1][1]};
      *(LAS f32x4*)(stg + fr * 128 + (((4 * i + q) ^ (fr & 7)) * 16)) = o;
    }
#pragma unroll
    for (int ii = 0; ii < 2; ++ii) {
      const int row = 8 * ii + (lane >> 3), ck = lane & 7;
      const f32x4 v = *(LAS f32x4*)(stg + row * 128 + ((ck ^ (row & 7)) * 16));
      *(f32x4*)(p.out + (64 * r + 16 * tt + row) * DM + 64 * c + 32 * np + ck * 4) = v;
    }
  }
  STAMP(15);
}

__global__ void __launch_bounds__(768, 3) fused_kernel(Params p) {
  extern __shared__ __attribute__((aligned(16))) char smem[];
  LAS char* lds = (LAS char*)smem;
  const int wave = __builtin_amdgcn_readfirstlane(threadIdx.x >> 6), lane = threadIdx.x & 63;
  const int g = blockIdx.x & 7, jx = blockIdx.x >> 3;
  const int r = (g >> 1) * 8 + (jx >> 2), c = (g & 1) * 4 + (jx & 3);
  const bool do5 = jx < 28;
  const int R5 = (g >> 1) * 4 + jx / 7, c5 = (g & 1) * 7 + jx % 7;
  if (wave >= 8) loader(p, lds, wave - 8, lane, r, c, R5, c5, do5);
  else compute(p, lds, wave, lane, r, c, R5, c5, do5);
}
}

extern "C" void kernel_launch(void* const* d_in, const int* in_sizes, int n_in, void* d_out, int out_size,
                              void* d_ws, size_t ws_size, hipStream_t stream) {
  (void)in_sizes; (void)n_in; (void)out_size; (void)ws_size;
  Params p;
  p.x = (const char*)d_in[0];
  p.lam_re = (const char*)d_in[1];
  p.lam_im = (const char*)d_in[2];
  p.b_re = (const char*)d_in[3];
  p.b_im = (const char*)d_in[4];
  p.c_re = (const char*)d_in[5];
  p.c_im = (const char*)d_in[6];
  p.log_dt = (const char*)d_in[7];
  p.dskip = (const char*)d_in[8];
  p.w1 = (const char*)d_in[10];
  p.w2 = (const char*)d_in[11];
  p.w3 = (const char*)d_in[12];
  p.w4 = (const char*)d_in[13];
  p.out = (float*)d_out;
  p.ws = (char*)d_ws;
  (void)hipFuncSetAttribute((const void*)fused_kernel, hipFuncAttributeMaxDynamicSharedMemorySize, LDS_BYTES);
  fused_kernel<<<256, 768, LDS_BYTES, stream>>>(p);
}
```

```cpp
#include <hip/hip_runtime.h>
#include <hip/hip_bf16.h>
#include <stdint.h>

typedef __attribute__((ext_vector_type(4))) short s16x4;
typedef __attribute__((ext_vector_type(8))) short s16x8;
typedef __attribute__((ext_vector_type(4))) float f32x4;
typedef __attribute__((ext_vector_type(2))) float f32x2;
typedef __attribute__((ext_vector_type(4))) unsigned u32x4;
typedef __attribute__((ext_vector_type(2))) unsigned u32x2;
typedef __attribute__((ext_vector_type(2))) __bf16 bf16x2v;
typedef unsigned long long u64;
#define LAS __attribute__((address_space(3)))
#define GAS __attribute__((address_space(1)))
#define DEVI __device__ __forceinline__

namespace {
constexpr int SEQ = 2048, DM = 512, FF = 1792;
constexpr float EPS = 1e-5f;
constexpr unsigned WS_EPOCH = 0, WS_TMO = 2048;
constexpr unsigned FS = 256;
constexpr unsigned WS_F1 = 65536, WS_F2 = WS_F1 + 256 * FS, WS_F3 = WS_F2 + 256 * FS, WS_F4 = WS_F3 + 256 * FS,
                   WS_F5 = WS_F4 + 256 * FS;
constexpr unsigned WS_H1 = 768u << 10;
constexpr unsigned H1K = 130 * 1024;
constexpr unsigned WS_UT = 3u << 20;
constexpr unsigned WS_X1 = WS_UT + (4u << 20);
constexpr unsigned WS_ST3 = WS_X1 + (2u << 20);
constexpr unsigned WS_HT = WS_ST3 + (1u << 20);
constexpr unsigned WS_HID = WS_HT + (4u << 20);
constexpr unsigned WS_END = WS_HID + (8u << 20);
constexpr int SLOT = 16384;
constexpr int L_X = 4 * SLOT;
constexpr int L_STG = 8 * SLOT;
constexpr int L_FLG = L_STG + 8 * 2048;
constexpr int L_P1 = L_FLG + 3072;
constexpr int L_ST1 = L_P1 + 1536;
constexpr int L_ST5 = L_ST1 + 512;
constexpr int L_SCN = L_ST5 + 2048;
constexpr int L_CTL = L_SCN + 64;
constexpr int L_CNT = L_CTL;
constexpr int L_SF = L_CTL + 32;
constexpr int LDS_BYTES = L_CTL + 96;
constexpr int NSTAGE = 81;
constexpr int PUB4 = 22, PUB5 = 40;
constexpr int J0_3 = 9, J0_4 = 18, J0_6 = 31, J0_7 = 47, JR_6 = 28, JR_7 = 45, TRY3 = 2, TRY4 = 1;
constexpr int START_LIM = 2, BURST = 3, EARLY3 = 16, PF_ITA = 7, PF_ITB = 2, CODE_CH = 1664;
constexpr int POS_ST = 26, IT_ST = 27;
constexpr unsigned LDS_SPIN_CAP = 1u << 22;
constexpr unsigned SPIN_CAP = 1u << 18;

struct Params {
  const char *x, *lam_re, *lam_im, *b_re, *b_im, *c_re, *c_im, *log_dt, *dskip, *w1, *w2, *w3, *w4;
  float* out;
  char* ws;
};

DEVI float as_f(unsigned u) { return __builtin_bit_cast(float, u); }
DEVI unsigned as_u(float f) { return __builtin_bit_cast(unsigned, f); }
DEVI float bf_lo(unsigned u) { return as_f(u << 16); }
DEVI float bf_hi(unsigned u) { return as_f(u & 0xffff0000u); }
DEVI unsigned pk2(f32x2 v) { return __builtin_bit_cast(unsigned, __builtin_convertvector(v, bf16x2v)); }
DEVI f32x2 unpk(unsigned u) { return f32x2{bf_lo(u), bf_hi(u)}; }
DEVI f32x2 lo2(f32x4 v) { return f32x2{v[0], v[1]}; }
DEVI f32x2 hi2(f32x4 v) { return f32x2{v[2], v[3]}; }
DEVI float rsq(float v) { return __builtin_amdgcn_rsqf(v); }
DEVI f32x2 rcp1p_exp2(f32x2 e) {
  const f32x2 d = f32x2{__builtin_amdgcn_exp2f(e[0]), __builtin_amdgcn_exp2f(e[1])} + 1.0f;
  return f32x2{__builtin_amdgcn_rcpf(d[0]), __builtin_amdgcn_rcpf(d[1])};
}
DEVI u32x2 gelu_pk4(f32x4 w, float k1, float k2, float ir) {
  const f32x4 a = w * (w * w * k2 + k1);
  const f32x4 e = {__builtin_amdgcn_exp2f(a[0]), __builtin_amdgcn_exp2f(a[1]), __builtin_amdgcn_exp2f(a[2]), __builtin_amdgcn_exp2f(a[3])};
  const f32x4 d = e * ir + ir;
  const f32x4 g = w * f32x4{__builtin_amdgcn_rcpf(d[0]), __builtin_amdgcn_rcpf(d[1]), __builtin_amdgcn_rcpf(d[2]), __builtin_amdgcn_rcpf(d[3])};
  return u32x2{pk2(lo2(g)), pk2(hi2(g))};
}
constexpr float GC1 = -2.3022081983f, GC2 = -0.1029432397f;
DEVI f32x2 silu2(f32x2 z) { return z * rcp1p_exp2(z * -1.4426950409f); }
template <class T>
DEVI void pin(T& v) { asm volatile("" : "+v"(v)); }
DEVI f32x4 mfma(s16x8 a, s16x8 b, f32x4 c) { return __builtin_amdgcn_mfma_f32_16x16x32_bf16(a, b, c, 0, 0, 0); }
template <int A, int B>
DEVI void pin_all(f32x4 (&acc)[A][B]) {
#pragma unroll
  for (int i = 0; i < A; ++i)
#pragma unroll
    for (int j = 0; j < B; ++j) pin(acc[i][j]);
}
DEVI LAS char* lp(int a) { return (LAS char*)(size_t)(unsigned)a; }
DEVI s16x8 lds_b128(LAS char* p) { return *(LAS s16x8*)p; }
DEVI s16x8 lds_tr2(LAS char* p0, LAS char* p1) {
  s16x4 a = __builtin_amdgcn_ds_read_tr16_b64_v4i16((LAS s16x4*)p0);
  s16x4 b = __builtin_amdgcn_ds_read_tr16_b64_v4i16((LAS s16x4*)p1);
  return __builtin_shufflevector(a, b, 0, 1, 2, 3, 4, 5, 6, 7);
}
DEVI int hswz(int k) { return ((k >> 1) & 1) | ((((k >> 2) ^ (k >> 3)) & 1) << 1); }

template <bool SC1>
DEVI void glds4(const char* base, unsigned dst, unsigned v0, unsigned v1, unsigned v2, unsigned v3) {
  if constexpr (SC1)
    asm volatile("s_mov_b32 m0, %5\n\ts_nop 0\n\t"
                 "global_load_lds_dwordx4 %0, %4 sc1\n\tglobal_load_lds_dwordx4 %1, %4 offset:1024 sc1\n\t"
                 "global_load_lds_dwordx4 %2, %4 offset:2048 sc1\n\tglobal_load_lds_dwordx4 %3, %4 offset:3072 sc1"
                 ::"v"(v0), "v"(v1), "v"(v2), "v"(v3), "s"(base), "s"(dst) : "memory");
  else
    asm volatile("s_mov_b32 m0, %5\n\ts_nop 0\n\t"
                 "global_load_lds_dwordx4 %0, %4\n\tglobal_load_lds_dwordx4 %1, %4 offset:1024\n\t"
                 "global_load_lds_dwordx4 %2, %4 offset:2048\n\tglobal_load_lds_dwordx4 %3, %4 offset:3072"
                 ::"v"(v0), "v"(v1), "v"(v2), "v"(v3), "s"(base), "s"(dst) : "memory");
}
template <bool SC1>
DEVI void glds4(const char* base, unsigned dst, const unsigned (&v)[4]) { glds4<SC1>(base, dst, v[0], v[1], v[2], v[3]); }
DEVI void warm(const char* base, unsigned voff, unsigned dump) {
  asm volatile("s_mov_b32 m0, %2\n\ts_nop 0\n\tglobal_load_lds_dwordx4 %0, %1" ::"v"(voff), "s"(base), "s"(dump) : "memory");
}
DEVI u64 ld_flag(const char* p) {
  return __hip_atomic_load((const GAS u64*)p, __ATOMIC_RELAXED, __HIP_MEMORY_SCOPE_AGENT);
}
DEVI void st_flag(char* p, u64 v) {
  (void)__hip_atomic_exchange((GAS u64*)p, v, __ATOMIC_RELAXED, __HIP_MEMORY_SCOPE_AGENT);
}
DEVI bool vm_idle() { return (__builtin_amdgcn_s_getreg((31 << 11) | 7) & 0x00c0000fu) == 0u; }
template <int NREP = 1>
DEVI void publish(unsigned cnt_addr, unsigned nwaves, char* flag, u64 tag, int lane) {
  asm volatile("s_waitcnt vmcnt(0)" ::: "memory");
  if (lane == 0) {
    unsigned old;
    asm volatile("ds_add_rtn_u32 %0, %1, %2\n\ts_waitcnt lgkmcnt(0)" : "=v"(old) : "v"(cnt_addr), "v"(1u) : "memory");
    if (old == nwaves - 1u) {
#pragma unroll
      for (int k = 0; k < NREP; ++k) st_flag(flag + k * 2048, tag);
    }
  }
}
constexpr DEVI int slot_idx(int u) { return u < 12 ? (u & 3) : (int)((0x73216054u >> (4 * ((u - 12) & 7))) & 7u); }
DEVI int slot_of(int u) { return slot_idx(u) * SLOT; }

template <bool ISA, bool DO5>
struct Sched {
  static constexpr int skip = DO5 ? 0 : 16, npos = NSTAGE - skip;
  static constexpr int stage(int u) { return u < 21 ? u : u + skip; }
  static constexpr int type(int u) {
    const int s = stage(u);
    return s < 4 ? 1 : s < 12 ? 2 : s < 20 ? 3 : s < 21 ? 4 : s < 37 ? 5 : s < 53 ? 6 : 7;
  }
  static constexpr int first(int t) {
    return t == 1 ? 0 : t == 2 ? 4 : t == 3 ? 12 : t == 4 ? 20 : t == 5 ? 21 : t == 6 ? 37 - skip : t == 7 ? 53 - skip : npos;
  }
  static constexpr int cnt(int u) { return u == 0 ? 0 : u < 4 ? 8 : (u >= 8 && u < 12) ? (ISA ? 0 : 8) : u == 20 ? (ISA ? 8 : 0) : (ISA && DO5 && u == POS_ST) ? 12 : 4; }
  static constexpr bool stats_at(int j) { return !ISA && DO5 && j >= IT_ST && j < IT_ST + 4; }
  static constexpr int prev(int u) {
    if (u < 12) return u - 4;
    if (u >= 20) return u - 8;
    const int s = slot_idx(u);
    return s < 4 ? 8 + s : 5 + 2 * (s - 4);
  }
  static constexpr bool xbar(int j) { return j == 4 || j == 12 || j == 20 || (DO5 && j == 37); }
  static constexpr bool slot_free(int u, int j, bool behind_x) {
    if (prev(u) < 0 || j >= prev(u) + (type(prev(u)) == 2 || type(prev(u)) == 4 ? 2 : 3)) return true;
    return (behind_x && xbar(j) && prev(u) < j) || (xbar(j - 1) && prev(u) < j - 1);
  }
  static constexpr int j0(int t) {
    return t == 3 ? J0_3 : t == 4 ? J0_4 : (t == 6 && DO5) ? J0_6 : t == 7 ? J0_7 - skip : -1;
  }
  static constexpr int jr(int t) { return j0(t) < 0 ? -1 : (t == 6 && DO5) ? JR_6 : (t == 7 && DO5) ? JR_7 : j0(t); }
  static constexpr int j1(int t) { return j0(t) >= 0 ? j0(t) + 1 : first(t); }
  static constexpr bool entry(int j) { return ISA && type(j) >= 3 && j == first(type(j)); }
  struct Tab {
    int lim[NSTAGE + 1];
    constexpr Tab() : lim() {
      int u = -1;
      for (int j = 0; j < npos; ++j) {
        const int cap = j == 0 ? START_LIM : (!ISA && !DO5 && j < 21) ? 20 : !ISA ? npos : (DO5 && j == 37) ? npos : entry(j) ? (u + 1 > j + BURST - 1 ? u + 1 : j + BURST - 1) : (j < 12 && u + 2 > EARLY3) ? EARLY3 : u + 2;
        while (u + 1 < npos && (cnt(u + 1) == 0 || slot_free(u + 1, j, true)) && !(ISA && type(u + 1) >= 3 && j < j1(type(u + 1))) && (u + 1 <= cap || cnt(u + 1) == 0))
          ++u;
        lim[j] = u;
      }
    }
  };
  static constexpr Tab tab{};
  static constexpr int lim(int j) { return j < 0 ? START_LIM : tab.lim[j]; }
  static constexpr int lim_pre(int j) {
    int u = lim(j - 1);
    if (entry(j)) return u;
    while (u + 1 <= lim(j) && (cnt(u + 1) == 0 || slot_free(u + 1, j, false))) ++u;
    return u;
  }
  static constexpr int req_at(int j) {
    for (int t = 3; t <= 7; ++t) if (ISA && jr(t) == j) return t;
    return 0;
  }
  static constexpr int early(int j) {
    for (int t = 3; t <= 7; ++t) if (ISA && j0(t) >= 0 && j1(t) <= j && j < first(t)) return t;
    return 0;
  }
  static constexpr int nfl(int) { return 1; }
  static constexpr int waitn(int j) {
    int n = 0;
    const int last = early(j) ? first(early(j)) - 1 : lim(j);
    for (int u = j + 1; u <= last; ++u) n += cnt(u);
    if (early(j)) n += nfl(early(j));
    for (int t = 3; t <= 7; ++t) if (ISA && jr(t) >= 0 && jr(t) <= j && j <= j0(t) && j <= lim(jr(t))) n += nfl(t);
    constexpr int pf = ISA ? PF_ITA : PF_ITB;
    if (j >= pf && j <= lim(pf)) n += ISA ? 6 : 3;
    return n;
  }
  static constexpr int wait_ok(int j) {
    int n = 0;
    for (int u = j + 1; u <= lim(j); ++u) n += cnt(u);
    return n;
  }
  static constexpr int younger(int t) {
    int n = 0;
    for (int u = lim(jr(t)) + 1; u < first(t); ++u) n += cnt(u);
    return n;
  }
  static constexpr bool single(int j) {
    return lim(j) == lim(j - 1) + 1 && cnt(lim(j)) == 4 && !entry(j) && !req_at(j) && !early(j) && !xbar(j) &&
           !stats_at(j) && j != (ISA ? PF_ITA : PF_ITB);
  }
  static constexpr int runlen(int j) {
    if (!single(j)) return 1;
    int n = 1;
    while (j + n < npos && single(j + n) && type(lim(j + n)) == type(lim(j)) && waitn(j + n) == waitn(j)) ++n;
    return n;
  }
  static constexpr bool valid() {
    for (int t = 3; t <= 7; ++t) {
      if (t == 5 && !DO5) continue;
      if (j0(t) < 0) continue;
      if (!(jr(t) <= j0(t) && j1(t) <= first(t)) || (jr(t) < j0(t) && j1(t) == first(t))) return false;
      if (ISA && lim(j0(t)) != first(t) - 1) return false;
    }
    for (int j = 0; j < npos; ++j) if (lim(j) < j || lim(j) < lim(j - 1) || waitn(j) > 63) return false;
    return true;
  }
};

struct LoadCtx {
  const Params* p;
  unsigned lds;
  unsigned vH[2], vX[4], vK1[4], vK4[4], vM[4], vW3[4], vL;
  int lw, lane, r, c, R5, c5;
  bool ok;
  u64 tag, act;
  f32x2 pv[16], sm;
};

template <int N>
DEVI void wait_bar() {
  asm volatile("s_waitcnt vmcnt(%0)\n\ts_barrier" ::"i"(N) : "memory");
}

template <bool ISA, int T>
DEVI void issue(const LoadCtx& x, int u, int q) {
  const Params& p = *x.p;
  const int lw = x.lw, half = lw & 1, r = x.r, c = x.c;
  const unsigned dst = x.lds + slot_of(u) + lw * 4096;
  [[maybe_unused]] const unsigned vL = T > 2 ? x.vL : 0u;
  if constexpr (T == 1) {
    glds4<false>(p.x + (64 * r + 16 * lw) * 1024 + q * 256, dst + L_X, x.vX);
    glds4<false>(p.w1 + (128 * q + 32 * lw) * 1024 + c * 128, dst, x.vK1);
  } else if constexpr (T == 2) {
    if (!ISA && q >= 4) glds4<false>(p.b_re + (64 * c + 32 * half) * 1024 + q * 128, dst - 8192, x.vM);
    glds4<false>((ISA ? p.b_re : p.b_im) + (64 * c + 32 * half) * 1024 + q * 128, dst, x.vM);
  } else if constexpr (T == 3) {
    if constexpr (ISA) {
      const unsigned v = q < 4 ? x.vH[0] : x.vH[1];
      glds4<true>(p.ws + WS_H1 + (q + 8 * half) * H1K + r * 4096, dst, v, v, v, v);
    }
    else glds4<false>(p.w2 + 32 * (q + 8 * half) * 1024 + c * 128, dst, x.vK1);
  } else if constexpr (T == 4) {
    const char* b = p.ws + WS_UT + (int)blockIdx.x * 8192 + lw * 4096;
    glds4<true>(b, dst, vL, vL, vL, vL);
    glds4<true>(b + 256 * 8192, dst + 8192, vL, vL, vL, vL);
  } else if constexpr (T == 5) {
    if constexpr (ISA) glds4<true>(p.ws + WS_X1 + (x.R5 * 16 + q) * 8192 + half * 4096, dst, vL, vL, vL, vL);
    else glds4<false>(p.w3 + (32 * q + 16 * half) * (FF * 2) + x.c5 * 256, dst, x.vW3);
  } else if constexpr (T == 6) {
    if constexpr (ISA) glds4<true>(p.ws + WS_HT + (64 * q + 32 * half) * 4096 + r * 128, dst, x.vK4);
    else glds4<false>((q < 8 ? p.c_re : p.c_im) + (64 * c + 32 * half) * 1024 + (q & 7) * 128, dst, x.vM);
  } else {
    if constexpr (ISA) glds4<true>(p.ws + WS_HID + (r * 56 + q + 28 * half) * 4096, dst, vL, vL, vL, vL);
    else glds4<false>(p.w4 + 32 * (q + 28 * half) * 1024 + c * 128, dst, x.vK1);
  }
}

template <int T>
DEVI void flags_request(LoadCtx& x) {
  const int lane = x.lane;
  unsigned fo;
  int n = 64;
  if constexpr (T == 3) {
    const int cc = 4 * x.lw + (lane & 3);
    int rr = x.r + ((lane & 4) ? ((cc & 2) ? 1 : -1) : 0);
    rr = rr < 0 ? 0 : rr > 31 ? 31 : rr;
    fo = WS_F1 + FS * (rr * 8 + cc);
    n = 8;
  } else if constexpr (T == 4) {
    fo = WS_F2 + FS * (((int)blockIdx.x >> 5) * 32 + 16 * x.lw + lane);
    n = 16;
  } else if constexpr (T == 5) {
    fo = WS_F3 + FS * (x.R5 * 16 + 8 * x.lw + lane);
    n = 8;
  } else if constexpr (T == 6) {
    fo = WS_F4 + ((int)blockIdx.x & 7) * 2048 + x.lw * 1024 + lane * 16;
  } else {
    fo = WS_F5 + FS * ((x.r >> 1) * 16 + 7 * x.lw + lane);
    n = 7;
  }
  x.act = n == 64 ? ~0ull : (1ull << n) - 1ull;
  if constexpr (T == 6) {
    asm volatile("v_mov_b64 v[164:165], %2\n\tglobal_load_dwordx4 v[164:167], %0, %1 sc1"
                 ::"v"(fo), "s"(x.p->ws), "s"(~x.tag) : "v164", "v165", "v166", "v167", "memory");
  } else {
    const u64 z = 0;
    asm volatile("v_mov_b64 v[164:165], %4\n\ts_mov_b64 exec, %3\n\t"
                 "global_atomic_add_x2 v[164:165], %0, %1, %2 sc0 sc1\n\ts_mov_b64 exec, -1"
                 ::"v"(fo), "v"(z), "s"(x.p->ws), "s"(x.act), "s"(~x.tag) : "v164", "v165", "memory");
  }
}
template <int T, int N = 0>
DEVI bool flags_match(const LoadCtx& x) {
  u64 f0, f1;
  if constexpr (T == 6) {
    asm volatile("s_waitcnt vmcnt(%2)\n\tv_mov_b64 %0, v[164:165]\n\tv_mov_b64 %1, v[166:167]" : "=&v"(f0), "=&v"(f1) : "i"(N) : "memory");
  } else {
    asm volatile("s_waitcnt vmcnt(%1)\n\tv_mov_b64 %0, v[164:165]" : "=v"(f0) : "i"(N) : "memory");
    f1 = f0;
  }
  return (__ballot(f0 == x.tag && f1 == x.tag) & x.act) == x.act;
}
DEVI void st_flag_lane0(const char* base, unsigned off, u64 v) {
  asm volatile("s_mov_b64 exec, 1\n\tglobal_atomic_swap_x2 %0, %1, %2 sc1\n\ts_mov_b64 exec, -1" ::"v"(off), "v"(v), "s"(base) : "memory");
}
template <int T, bool PENDING>
DEVI void flags_wait(LoadCtx& x) {
  char* const ws = x.p->ws;
  if constexpr (!PENDING) flags_request<T>(x);
#pragma unroll 1
  for (unsigned spins = 0;; ++spins) {
    if (flags_match<T>(x)) break;
    __builtin_amdgcn_s_sleep(T == 6 ? 8 : 1);
    if ((spins & 63u) == 63u && __all(ld_flag(ws + WS_TMO) == x.tag)) break;
    if (spins > SPIN_CAP) {
      st_flag_lane0(ws, WS_TMO, x.tag);
      break;
    }
    flags_request<T>(x);
  }
}

template <bool ISA, bool DO5, int U, int UEND>
DEVI void issue_range(const LoadCtx& x) {
  using S = Sched<ISA, DO5>;
  if constexpr (U <= UEND) {
    constexpr int t = S::type(U), q = U - S::first(t);
    if constexpr (S::cnt(U) > 0) issue<ISA, t>(x, U, q);
    if constexpr (S::cnt(U) == 12) {
      const unsigned v0 = (x.lane >> 5) * (SEQ * 8) + (x.lane & 31) * 16, d = 2 * SEQ * 8 - 1024;
      const char* b = x.p->ws + WS_ST3 + (128 * x.R5 + 64 * x.lw) * 8;
      glds4<true>(b, x.lds + L_STG + x.lw * 8192, v0, v0 + d, v0 + 2 * d, v0 + 3 * d);
      glds4<true>(b + 8 * SEQ * 8, x.lds + L_STG + x.lw * 8192 + 4096, v0, v0 + d, v0 + 2 * d, v0 + 3 * d);
    }
    issue_range<ISA, DO5, U + 1, UEND>(x);
  }
}

DEVI void setup_rest(LoadCtx& x) {
  int lane = x.lane;
  const int lw = x.lw;
  pin(lane);
  x.lane = lane;
#pragma unroll
  for (int i = 0; i < 4; ++i) {
    const int row = 8 * i + (lane >> 3), ch = lane & 7;
    x.vK4[i] = row * 4096 + (((((ch >> 1) ^ hswz(row)) << 1) | (ch & 1)) * 16) - i * 1024;
    x.vM[i] = row * 1024 + ((ch ^ ((row >> 1) & 7)) * 16) - i * 1024;
    const int row3 = 4 * i + (lane >> 4), ch3 = lane & 15;
    x.vW3[i] = row3 * (FF * 2) + (((((ch3 >> 1) ^ (row3 & 7)) << 1) | (ch3 & 1)) * 16) - i * 1024;
  }
  x.vL = lane * 16;
#pragma unroll
  for (int i = 0; i < 2; ++i) {
    const int row = (lane & 15) + (i ? 1 + (lw & 1) : -1 - (lw & 1));
    x.vH[i] = ((row >> 4) + 1) * 1024 + (lane >> 4) * 256 + (row & 15) * 16;
  }
#pragma unroll
  for (int i = 0; i < 4; ++i) { pin(x.vK4[i]); pin(x.vM[i]); pin(x.vW3[i]); }
  pin(x.vL); pin(x.vH[0]); pin(x.vH[1]);
}

template <bool ISA, bool DO5, int J>
DEVI void run_from(LoadCtx& x) {
  using S = Sched<ISA, DO5>;
  if constexpr (J == 0) static_assert(S::valid(), "loader schedule");
  if constexpr (J < S::npos) {
    constexpr int n = S::runlen(J);
    if constexpr (n > 1) {
      constexpr int u0 = S::lim(J), t = S::type(u0), f0 = S::first(t);
#pragma unroll 1
      for (int u = u0; u < u0 + n; ++u) {
        issue<ISA, t>(x, u, u - f0);
        wait_bar<S::waitn(J)>();
      }
    } else {
      [[maybe_unused]] const Params& p = *x.p;
      [[maybe_unused]] const int lane = x.lane;
      constexpr int done = S::xbar(J) ? S::lim_pre(J) : S::lim(J - 1);
      if constexpr (S::xbar(J)) {
        static_assert(S::early(J) == 0, "schedule");
        issue_range<ISA, DO5, S::lim(J - 1) + 1, done>(x);
        asm volatile("s_barrier" ::: "memory");
      }
      if constexpr (S::entry(J)) {
        constexpr int t = S::type(J);
        constexpr bool window = S::j0(t) >= 0;
        if (!window || !x.ok) {
          flags_wait<t, window>(x);
          issue_range<ISA, DO5, J, done>(x);
        }
        if constexpr (t == 7) {
          if (blockIdx.x == 0 && x.lw == 0) st_flag_lane0(p.ws, WS_EPOCH, x.tag);
        }
      }
      if constexpr (S::early(J) != 0) {
        constexpr int t = S::early(J);
        if constexpr (S::jr(t) < S::j0(t) && J == S::j1(t)) {
          x.ok = flags_match<t, S::younger(t)>(x);
          if (!x.ok) flags_request<t>(x);
        } else if (!x.ok) {
          constexpr int tries = t == 3 ? TRY3 : t == 4 ? TRY4 : 1;
#pragma unroll 1
          for (int k = 0; k < tries && !x.ok; ++k) {
            if (vm_idle()) {
              x.ok = flags_match<t>(x);
              if (!x.ok) flags_request<t>(x);
            }
          }
          if (x.ok) {
            issue_range<ISA, DO5, S::first(t), done>(x);
          }
        }
        if (x.ok) {
          issue_range<ISA, DO5, done + 1, S::lim(J)>(x);
          wait_bar<S::wait_ok(J)>();
        } else {
          wait_bar<S::waitn(J)>();
        }
      } else {
        issue_range<ISA, DO5, done + 1, S::lim(J)>(x);
        if constexpr (J == 1) setup_rest(x);
        if constexpr (J == (ISA ? PF_ITA : PF_ITB)) {
          const unsigned dump = x.lds + L_FLG + 2048, b = blockIdx.x;
          if constexpr (ISA) {
            const char* m = p.w3 + b * 7168 + x.lw * 3072;
#pragma unroll
            for (int k = 0; k < 4; ++k) warm(m + k * 1024, x.vL, dump);
            const char* mc = (x.lw ? p.c_im : p.c_re) + b * 2048;
            warm(mc, x.vL, dump);
            warm(mc + 1024, x.vL, dump);
          } else {
            const char* m = ((x.lw & 1) ? p.b_im : p.b_re) + b * 2048;
            warm(m, x.vL, dump);
            warm(m + 1024, x.vL, dump);
            warm(p.w2 + b * 2048 + (x.lw & 1) * 1024, x.vL, dump);
          }
        }
        if constexpr (J == 4 && ISA) {
          asm volatile("s_load_dwordx2 %0, %1, 0x0\n\ts_waitcnt lgkmcnt(0)" : "=s"(x.tag) : "s"(p.ws) : "memory");
          x.tag += 1ull;
        }
        if constexpr (S::stats_at(J)) {
          if constexpr (J == IT_ST) {
            __builtin_amdgcn_s_setprio(3);
#pragma unroll
            for (int k = 0; k < 16; ++k) x.pv[k] = *(LAS f32x2*)(lp(x.lds + L_STG + (x.lw & 1) * 8192 + lane * 8) + k * 512);
          } else if constexpr (J == IT_ST + 1) {
            x.sm = x.pv[0];
#pragma unroll
            for (int k = 1; k < 16; ++k) x.sm += x.pv[k];
            x.sm[0] *= 1.0f / 16.0f;
            pin(x.sm);
          } else if constexpr (J == IT_ST + 2) {
            float dv = 0.f, dw = 0.f;
#pragma unroll
            for (int k = 0; k < 16; k += 2) {
              const float d = x.pv[k][0] - x.sm[0], e = x.pv[k + 1][0] - x.sm[0];
              dv = fmaf(d, d, dv);
              dw = fmaf(e, e, dw);
            }
            x.sm[1] = fmaf(32.0f, dv + dw, x.sm[1]);
            pin(x.sm);
          } else {
            LAS float* st = (LAS float*)lp(x.lds + L_ST5 + (64 * (x.lw & 1) + lane) * 4);
            const float rstd = rsq(x.sm[1] * (1.0f / DM) + EPS);
            st[0] = -x.sm[0];
            st[128] = GC1 * rstd;
            st[256] = GC2 * rstd * rstd * rstd;
            st[384] = __builtin_amdgcn_rcpf(rstd);
            asm volatile("s_waitcnt lgkmcnt(0)" ::: "memory");
            __builtin_amdgcn_s_setprio(0);
          }
        }
        if constexpr (S::req_at(J) != 0) {
          x.ok = false;
          flags_request<S::req_at(J)>(x);
        }
        wait_bar<S::waitn(J)>();
      }
    }
    run_from<ISA, DO5, J + n>(x);
  }
}

DEVI void loader(const Params& p, LAS char* lds, int lw, int lane, int r, int c, int R5, int c5, bool do5) {
  LoadCtx x;
  x.p = &p; x.lds = (unsigned)(size_t)lds; x.lw = lw; x.lane = lane; x.r = r; x.c = c; x.R5 = R5; x.c5 = c5; x.tag = 0; x.ok = false; x.act = 0;
#pragma unroll
  for (int i = 0; i < 4; ++i) {
    const int row = 8 * i + (lane >> 3), ch = lane & 7;
    x.vK1[i] = row * 1024 + (((((ch >> 1) ^ hswz(row)) << 1) | (ch & 1)) * 16) - i * 1024;
    const int rx = 4 * i + (lane >> 4);
    x.vX[i] = rx * 1024 + (((lane & 15) ^ rx) * 16) - i * 1024;
  }
#pragma unroll
  for (int u = 1; u <= START_LIM; ++u) issue<false, 1>(x, u, u);
  if (lw < 2) {
    if (do5) run_from<true, true, 0>(x);
    else run_from<true, false, 0>(x);
  } else {
    if (do5) run_from<false, true, 0>(x);
    else run_from<false, false, 0>(x);
  }
}

DEVI float quad_col_sum(float v) {
  const auto r = __builtin_amdgcn_permlane32_swap(as_u(v), as_u(v), false, false);
  const unsigned r0 = r[0], r1 = r[1];
  v = as_f(r0) + as_f(r1);
  const auto s = __builtin_amdgcn_permlane16_swap(as_u(v), as_u(v), false, false);
  const unsigned s0 = s[0], s1 = s[1];
  return as_f(s0) + as_f(s1);
}
template <int CTRL, int ROWMASK = 0xf>
DEVI float dpp0(float v) {
  return as_f(__builtin_amdgcn_update_dpp(0u, as_u(v), CTRL, ROWMASK, 0xf, true));
}
template <int CTRL, int ROWMASK = 0xf>
DEVI f32x2 dpp0c(f32x2 v) { return f32x2{dpp0<CTRL, ROWMASK>(v[0]), dpp0<CTRL, ROWMASK>(v[1])}; }
DEVI f32x2 cmul(f32x2 a, f32x2 b) {
  return f32x2{fmaf(a[0], b[0], -(a[1] * b[1])), fmaf(a[0], b[1], a[1] * b[0])};
}
DEVI f32x2 cfma(f32x2 a, f32x2 b, f32x2 c) {
  return f32x2{fmaf(a[0], b[0], fmaf(-a[1], b[1], c[0])), fmaf(a[0], b[1], fmaf(a[1], b[0], c[1]))};
}

template <int NA, int NB>
struct Frag { s16x8 a[NA], b[NB]; };
template <bool DRAIN>
DEVI void cbar(int j = 95) {
  __builtin_amdgcn_sched_barrier(0);
  if (j == 0 || j == PUB4 || j == PUB5) asm volatile("s_waitcnt vmcnt(0)" ::: "memory");
  if constexpr (DRAIN) asm volatile("s_waitcnt lgkmcnt(0)\n\ts_barrier" ::: "memory");
  else asm volatile("s_barrier" ::: "memory");
  __builtin_amdgcn_sched_barrier(0);
}
#define SB() __builtin_amdgcn_sched_barrier(0)
template <int NR, int NM, int PER>
DEVI void interleave() {
#pragma unroll
  for (int i = 0; i < NM; ++i) {
    if (i * PER < NR) __builtin_amdgcn_sched_group_barrier(0x100, PER, 0);
    __builtin_amdgcn_sched_group_barrier(0x8, 1, 0);
  }
}
template <int N, int P0, int NR, int NM, int PER, class F, int NA, class LD, class MM, class AN, class HK>
DEVI void pipe(const int (&base)[NA], LD ld, MM mm, AN anchor, HK hook) {
  F f[2];
  int hi[NA];
#pragma unroll
  for (int i = 0; i < NA; ++i) { hi[i] = base[i] + 4 * SLOT; pin(hi[i]); }
#pragma unroll
  for (int u = 0; u < N; ++u) {
    if (u == 0) cbar<true>(P0);
    else cbar<false>(P0 + u);
    hook(-(P0 + u));
    if (u > 1) {
      anchor();
      SB();
    }
    const int sl = slot_idx(P0 + u);
    if (sl < 4) ld(f[u & 1], base, sl * SLOT);
    else ld(f[u & 1], hi, (sl - 4) * SLOT);
    if (u > 0) {
      mm(f[(u & 1) ^ 1], u == 1);
      interleave<NR, NM, PER>();
    }
    SB();
    hook(u);
  }
  SB();
  mm(f[(N - 1) & 1], false);
  anchor();
}
DEVI f32x4 mfma0(bool first, s16x8 a, s16x8 b, f32x4 c) {
  return mfma(a, b, first ? f32x4{0.f, 0.f, 0.f, 0.f} : c);
}
DEVI void xch_send(int a, const f32x4 (&acc)[2][2]) {
  *(LAS f32x4*)lp(a) = acc[0][1];
  *(LAS f32x4*)(lp(a) + 1024) = acc[1][1];
}

DEVI void compute(const Params& p, LAS char* lds, int cw, int lane, int r, int c, int R5, int c5, bool do5) {
  const int fr = lane & 15, q = lane >> 4;
  char* const ws = p.ws;
  const auto rs = __builtin_amdgcn_make_buffer_rsrc(ws, 0, (int)WS_END, 0x00020000);
  const int kh = cw >> 2, np = cw & 1, t2 = (cw >> 1) & 1, tt = 2 * t2 + kh;
  const int tok = 64 * r + 16 * tt + fr;
  {
    const unsigned base = (kh ? lane >> 3 : q) * 1024, sw = (kh ? (lane & 7) ^ (2 * q) : fr ^ q) * 16;
    unsigned v[4];
#pragma unroll
    for (int i = 0; i < 4; ++i) v[i] = base + (sw ^ (64 * (kh ? (i & 1) : i))) + i * (kh ? 7168 : 3072);
    const char* b = kh ? p.w1 + 32 * (cw & 3) * 1024 + c * 128 : p.x + (64 * r + 16 * cw) * 1024;
    const unsigned dst = (unsigned)(size_t)lds + (kh ? 0 : L_X) + (cw & 3) * 4096;
    glds4<false>(b, dst, v);
    SB();
  }

  const int chl = cw >> 2, wv = cw & 3, ch = 2 * (int)blockIdx.x + chl;
  u64 tag;
  unsigned plre, plim, pldt;
  if (cw == 1 && lane < 16) *((LAS unsigned*)(lds + L_CTL) + lane) = 0u;

  const int tq = fr >> 2, tp = fr & 3, lb = (int)(unsigned)(size_t)lds;
  const int xsw = (q ^ fr) * 16, ks32 = 2 * c + np;
  s16x8 ones = {0x3f80, 0x3f80, 0x3f80, 0x3f80, 0x3f80, 0x3f80, 0x3f80, 0x3f80};
  pin(ones);
  float mean_t, rstd_t;
  u32x2 xq[2];
  u32x4 x1p;
  const unsigned f4 = WS_F4 + (((int)blockIdx.x >> 4) & 1) * 1024 + (((int)blockIdx.x >> 5) * 16 + ((int)blockIdx.x & 15)) * 8;
  auto raise = [&](unsigned off) {
    if (cw == 0 && lane == 0) st_flag(ws + off, tag);
    SB();
  };
  bool pend = true;
  auto lazy_pub = [&](bool force, int which, unsigned off) {
    if (pend) {
      int n = 0;
      const int tries = which ? 1 : 4;
      while (!force && !vm_idle() && ++n < tries) {}
      if (n < tries) {
        publish(lb + L_CNT + 4 * which, 4u, ws + off, tag, lane);
        pend = false;
      }
    }
  };
  auto xq_addr = [&](int tile, int i) {
    const int cb = (64 * c + 32 * np + 16 * i + 4 * q) * 2;
    return lb + L_X + (cb >> 8) * SLOT + (16 * tile + fr) * 256 + ((cb & 255) ^ (fr * 16));
  };
  auto wait_scalars = [&] {
    asm volatile("s_load_dwordx2 %0, %4, 0x0\n\ts_load_dword %1, %5, %8\n\ts_load_dword %2, %6, %8\n\t"
                 "s_load_dword %3, %7, %8\n\ts_waitcnt lgkmcnt(0)"
                 : "=&s"(tag), "=&s"(plre), "=&s"(plim), "=&s"(pldt)
                 : "s"(ws), "s"(p.lam_re), "s"(p.lam_im), "s"(p.log_dt), "s"(4u * blockIdx.x) : "memory");
    tag += 1ull;
  };
  f32x2 a, coef, pw[7], pl, m16, m32;
  auto scan_setup = [&] {
    const float lre = bf_lo(plre >> (16 * chl)), lim = bf_lo(plim >> (16 * chl));
    const float dt = bf_lo(pk2(f32x2{__expf(bf_lo(pldt >> (16 * chl))), 0.f}));
    float rev = dt * lim * 0.15915494309189535f;
    rev -= __builtin_rintf(rev);
    const float mag = __expf(dt * lre);
    a = f32x2{mag * __builtin_amdgcn_cosf(rev), mag * __builtin_amdgcn_sinf(rev)};
    const float den = 2.0f / (lre * lre + lim * lim);
    coef = cmul(f32x2{a[0] - 1.0f, a[1]}, f32x2{lre * den, -lim * den});
    f32x2 t = cmul(a, a);
    t = cmul(t, t);
    pw[0] = cmul(t, t);
#pragma unroll
    for (int i = 1; i < 7; ++i) pw[i] = cmul(pw[i - 1], pw[i - 1]);
    pl = f32x2{1.0f, 0.0f};
#pragma unroll
    for (int i = 0; i < 6; ++i) {
      if (i == 4) m16 = cmul(pl, pw[0]);
      if (i == 5) m32 = cmul(pl, pw[0]);
      const f32x2 m = cmul(pl, pw[i]);
      if ((lane >> i) & 1) pl = m;
    }
    pin(pl); pin(m16); pin(m32); pin(a); pin(coef); pin(pw[0]); pin(pw[1]); pin(pw[2]); pin(pw[3]); pin(pw[6]);
  };
  using F22 = Frag<2, 2>;

  if (kh == 0) {
    int trN[2][2], xo[4], xqo[2];
#pragma unroll
    for (int t = 0; t < 2; ++t) {
      const int rn = 8 * q + 4 * t + tq;
#pragma unroll
      for (int i = 0; i < 2; ++i) {
        trN[i][t] = lb + rn * 128 + (((2 * np + i) ^ hswz(rn)) * 32) + tp * 8;
        xo[2 * t + i] = lb + L_X + (32 * t2 + fr) * 256 + ((64 * (2 * t + i)) ^ xsw);
        pin(trN[i][t]);
        pin(xo[2 * t + i]);
      }
      xqo[t] = xq_addr(2 * t2, t);
      pin(xqo[t]);
    }
    unsigned oh = WS_H1 + ks32 * H1K + (4 * r + 2 * t2 + 1) * 1024 + lane * 16;
    pin(oh);
    int aCr = lb + L_P1 + (32 * np + 4 * q) * 4, aT = lb + L_P1 + (32 * t2 + fr) * 4;
    pin(aCr); pin(aT);
    f32x4 acc[2][2];
    using F44 = Frag<4, 4>;
    auto mm = [&](const F44& g, bool first) {
#pragma unroll
      for (int h = 0; h < 2; ++h)
#pragma unroll
        for (int i = 0; i < 2; ++i)
#pragma unroll
          for (int m = 0; m < 2; ++m) acc[i][m] = mfma0(first && h == 0, g.a[2 * h + i], g.b[2 * h + m], acc[i][m]);
    };
    F44 f[2];
#pragma unroll
    for (int v = 0; v < 8; ++v) {
      const int u = v >> 1;
      if ((v & 1) == 0) cbar<false>(u);
      if (v == 2) {
        if ((r == 0 && t2 == 0) || (r == 31 && t2 == 1))
          __builtin_amdgcn_raw_buffer_store_b128(u32x4{0u, 0u, 0u, 0u}, rs, oh + (t2 ? 2048 : -1024), 0, 16);
        if (cw == 3 && lane < 5) {
          const unsigned fo = lane == 0 ? WS_F1 + (r * 8 + c) * FS : lane == 1 ? WS_F2 + (c * 32 + r) * FS
                            : lane == 2 ? WS_F3 + (r * 8 + c) * FS : lane == 3 ? f4 + ((int)blockIdx.x & 7) * 2048
                            : WS_F5 + ((R5 & 15) * 16 + c5) * FS;
          asm volatile("global_atomic_add_x2 %0, %1, %2 sc1" ::"v"(fo), "v"(0ull), "s"(ws) : "memory");
        }
        SB();
      }
      if (v > 1) { pin_all(acc); SB(); }
#pragma unroll
      for (int h = 0; h < 2; ++h) {
        const int ks = 2 * (v & 1) + h;
#pragma unroll
        for (int i = 0; i < 2; ++i) {
          f[v & 1].a[2 * h + i] = lds_tr2(lp(trN[i][0]) + u * SLOT + ks * 4096, lp(trN[i][1]) + u * SLOT + ks * 4096);
          f[v & 1].b[2 * h + i] = lds_b128(lp(xo[ks]) + u * SLOT + i * 4096);
        }
      }
      if (v > 0) {
        mm(f[(v & 1) ^ 1], v == 1);
        interleave<12, 8, 2>();
      }
      SB();
    }
    u32x2 xqm[2][2];
#pragma unroll
    for (int m = 0; m < 2; ++m)
#pragma unroll
      for (int i = 0; i < 2; ++i) xqm[m][i] = *(LAS u32x2*)(lp(xqo[i]) + 4096 * m);
    mm(f[1], false);
    cbar<true>(84);
    float tv[2][5];
    f32x4 cv[2];
#pragma unroll
    for (int m = 0; m < 2; ++m) {
      cv[m] = *(LAS f32x4*)(lp(aCr) + 64 * m);
#pragma unroll
      for (int k = 0; k < 5; ++k) tv[m][k] = *(LAS float*)(lp(aT) + 64 * m + (k < 3 ? 512 + 256 * k : 768 + 256 * k));
    }
    SB();
#pragma unroll
    for (int m = 0; m < 2; ++m) {
      u32x4 o;
#pragma unroll
      for (int i = 0; i < 2; ++i) {
        const u32x2 g = gelu_pk4(acc[i][m] - cv[i] * tv[m][3], tv[m][0], tv[m][1], tv[m][2]);
        o[2 * i] = g[0];
        o[2 * i + 1] = g[1];
      }
      __builtin_amdgcn_raw_buffer_store_b128(o, rs, oh + 1024 * m, 0, 16);
    }
    cbar<false>(4);
    int a3[5];
    f32x4 xn[2][2];
    unsigned ox, os;
#pragma unroll
    for (int j = 5; j < 12; ++j) {
      if (j == 6) wait_scalars();
      if (j == 5) {
#pragma unroll
        for (int m = 0; m < 2; ++m)
#pragma unroll
          for (int i = 0; i < 2; ++i) {
            const f32x2 x0 = unpk(xqm[m][i][0]), x1 = unpk(xqm[m][i][1]);
            xn[i][m] = (f32x4{x0[0], x0[1], x1[0], x1[1]} - tv[m][3]) * tv[m][4];
            pin(xn[i][m]);
          }
      }
      if (j == 11) {
#pragma unroll
        for (int t = 0; t < 2; ++t) {
          const int rp = 16 * t + 4 * q + tq;
#pragma unroll
          for (int i = 0; i < 2; ++i) {
            a3[2 * i + t] = lb + 8192 + rp * 128 + (((2 * np + i) ^ hswz(rp)) * 32) + tp * 8;
            pin(a3[2 * i + t]);
          }
        }
        a3[4] = lb + 2 * t2 * 1024 + lane * 16;
        ox = WS_X1 + ((tok >> 7) * 16 + ks32) * 8192 + ((tok >> 4) & 7) * 1024 + lane * 16;
        os = q == 0 ? WS_ST3 + (ks32 * SEQ + tok) * 8 : 0x80000000u;
        pin(a3[4]); pin(ox); pin(os);
      }
      if (j > 5 && j < 11) lazy_pub(j == 10, 0, WS_F1 + (r * 8 + c) * FS);
      cbar<false>(j);
    }
    cbar<false>(85);
    {
      int h3[5];
#pragma unroll
      for (int i = 0; i < 5; ++i) { h3[i] = a3[i] + 4 * SLOT; pin(h3[i]); }
      auto mm3 = [&](const F44& g) {
#pragma unroll
        for (int h = 0; h < 2; ++h)
#pragma unroll
          for (int i = 0; i < 2; ++i)
#pragma unroll
            for (int m = 0; m < 2; ++m) xn[i][m] = mfma(g.a[2 * h + i], g.b[2 * h + m], xn[i][m]);
      };
#pragma unroll
      for (int u = 0; u < 8; ++u) {
        const int sl = slot_idx(12 + u);
        cbar<false>(12 + u);
        if (u > 1) { pin_all(xn); SB(); }
#pragma unroll
        for (int h = 0; h < 2; ++h) {
          const int off = (sl & 3) * SLOT + h * 4096;
#pragma unroll
          for (int i = 0; i < 2; ++i) {
            f[u & 1].a[2 * h + i] = lds_tr2(lp(sl < 4 ? a3[2 * i] : h3[2 * i]) + off, lp(sl < 4 ? a3[2 * i + 1] : h3[2 * i + 1]) + off);
            f[u & 1].b[2 * h + i] = lds_b128(lp(sl < 4 ? a3[4] : h3[4]) + off + i * 1024);
          }
        }
        if (u > 0) {
          mm3(f[(u & 1) ^ 1]);
          interleave<12, 8, 2>();
        }
        SB();
      }
      mm3(f[1]);
    }
    cbar<true>(86);
    u32x4 xp[2];
#pragma unroll
    for (int m = 0; m < 2; ++m) {
      xp[m] = u32x4{pk2(lo2(xn[0][m])), pk2(hi2(xn[0][m])), pk2(lo2(xn[1][m])), pk2(hi2(xn[1][m]))};
      __builtin_amdgcn_raw_buffer_store_b128(xp[m], rs, ox + 1024 * m, 0, 16);
    }
#pragma unroll
    for (int m = 0; m < 2; ++m) {
      f32x2 v[4];
#pragma unroll
      for (int j = 0; j < 4; ++j) v[j] = unpk(xp[m][j]);
      const f32x2 s2 = (v[0] + v[1]) + (v[2] + v[3]);
      const float m32_ = quad_col_sum(s2[0] + s2[1]) * (1.0f / 32.0f);
      f32x2 q2 = {0.f, 0.f};
#pragma unroll
      for (int j = 0; j < 4; ++j) { const f32x2 d = v[j] - m32_; q2 += d * d; }
      const float m2 = quad_col_sum(q2[0] + q2[1]);
      __builtin_amdgcn_raw_buffer_store_b64(u32x2{as_u(m32_), as_u(m2)}, rs, os + 128 * m, 0, 16);
    }
    *(LAS u32x4*)(lds + L_STG + cw * 2048 + lane * 16) = xp[1];
    x1p = xp[0];
    xq[0] = xqm[0][0]; xq[1] = xqm[0][1];
    mean_t = tv[0][3]; rstd_t = tv[0][4];
    SB();
    scan_setup();
    publish(lb + L_CNT + 8, 4u, ws + WS_F3 + (r * 8 + c) * FS, tag, lane);
  } else {
    __builtin_amdgcn_s_setprio(1);
    wait_scalars();
    int trY[2], xo[4], xqo[2];
#pragma unroll
    for (int t = 0; t < 2; ++t) {
      const int rn = 8 * q + 4 * t + tq;
      trY[t] = lb + rn * 128 + ((wv ^ hswz(rn)) * 32) + tp * 8;
      xqo[t] = xq_addr(tt, t);
      pin(trY[t]); pin(xqo[t]);
    }
#pragma unroll
    for (int k = 0; k < 4; ++k) { xo[k] = lb + L_X + (16 * wv + fr) * 256 + ((64 * k) ^ xsw); pin(xo[k]); }
    const int th = cw & 1, ph = (cw >> 1) & 1;
    int xa[4], bo[2][2];
#pragma unroll
    for (int k = 0; k < 4; ++k) { xa[k] = lb + L_X + (32 * th + fr) * 256 + ((64 * k) ^ xsw); pin(xa[k]); }
#pragma unroll
    for (int ks = 0; ks < 2; ++ks)
#pragma unroll
      for (int h = 0; h < 2; ++h) {
        bo[ks][h] = lb + ph * 8192 + (32 * (h ^ th) + fr) * 128 + (((4 * ks + q) ^ ((fr >> 1) & 7)) * 16);
        pin(bo[ks][h]);
      }
    int aCw = lb + L_P1 + (16 * wv + 4 * q) * 4, aTw = lb + L_P1 + (16 * wv + fr) * 4;
    pin(aCw); pin(aTw);
    {
      f32x4 ex[1][3];
      F22 f[2];
      auto mm = [&](const F22& g, bool first) {
#pragma unroll
        for (int h = 0; h < 2; ++h) {
          ex[0][1] = mfma0(first && h == 0, ones, g.b[h], ex[0][1]);
          ex[0][2] = mfma0(first && h == 0, g.b[h], g.b[h], ex[0][2]);
          ex[0][0] = mfma0(first && h == 0, g.a[h], ones, ex[0][0]);
        }
      };
#pragma unroll
      for (int v = 0; v < 8; ++v) {
        const int u = v >> 1;
        if ((v & 1) == 0) cbar<false>(u);
        if (v > 1) { pin_all(ex); SB(); }
#pragma unroll
        for (int h = 0; h < 2; ++h) {
          const int ks = 2 * (v & 1) + h;
          f[v & 1].b[h] = lds_b128(lp(xo[ks]) + u * SLOT);
          f[v & 1].a[h] = lds_tr2(lp(trY[0]) + u * SLOT + ks * 4096, lp(trY[1]) + u * SLOT + ks * 4096);
        }
        if (v > 0) {
          mm(f[(v & 1) ^ 1], v == 1);
          interleave<6, 6, 1>();
        }
        SB();
      }
#pragma unroll
      for (int i = 0; i < 2; ++i) xq[i] = *(LAS u32x2*)lp(xqo[i]);
      mm(f[1], false);
      if (fr == 0) *(LAS f32x4*)lp(aCw) = ex[0][0];
      const float d01 = (tp & 1) ? ex[0][2][1] : ex[0][2][0], d23 = (tp & 1) ? ex[0][2][3] : ex[0][2][2];
      const float sq = (tp & 2) ? d23 : d01;
      const float mean = ex[0][1][0] * (1.0f / DM);
      const float var = fmaxf(fmaf(-mean, mean, sq * (1.0f / DM)), 0.f);
      const float rstd = rsq(var + EPS);
      const float r12 = rstd * rsq(var * rstd * rstd + EPS);
      if (tq == q) {
        *(LAS float*)(lp(aTw) + 512) = GC1 * r12;
        *(LAS float*)(lp(aTw) + 768) = GC2 * r12 * r12 * r12;
        *(LAS float*)(lp(aTw) + 1024) = __builtin_amdgcn_rcpf(r12);
        *(LAS float*)(lp(aTw) + 1536) = mean;
        *(LAS float*)(lp(aTw) + 1792) = rstd;
      }
    }
    __builtin_amdgcn_s_setprio(0);
    cbar<true>(84);
    int aM2 = lb + L_ST1 + (32 * th + 4 * q) * 4, aC2 = lb + L_P1 + (64 * ph + 32 * th + fr) * 4, aW2[2][2];
    int aR2 = lb + L_STG + wv * 4096 + (lane >> 2) * 64 + (((lane & 3) ^ ((lane >> 3) & 3)) * 16), aC3 = aC2 ^ 128;
    unsigned ou = WS_UT + (ph * 256 + 32 * c + (lane >> 3)) * 8192 + (2 * r + th) * 128 + (lane & 7) * 16;
#pragma unroll
    for (int h = 0; h < 2; ++h)
#pragma unroll
      for (int mt = 0; mt < 2; ++mt) {
        aW2[h][mt] = lb + L_STG + wv * 4096 + (32 * (h ^ th) + fr) * 64 + ((4 * mt + q) ^ (((fr >> 1) & 3) << 1)) * 8;
        pin(aW2[h][mt]);
      }
    f32x4 mr[2], rd[2];
#pragma unroll
    for (int mt = 0; mt < 2; ++mt) {
      rd[mt] = *(LAS f32x4*)(lp(aM2) + 256 + 64 * mt);
      mr[mt] = -*(LAS f32x4*)(lp(aM2) + 64 * mt) * rd[mt];
      pin(rd[mt]); pin(mr[mt]);
    }
    mean_t = *(LAS float*)(lds + L_ST1 + (16 * tt + fr) * 4);
    rstd_t = *(LAS float*)(lds + L_ST1 + 256 + (16 * tt + fr) * 4);
    pin(aC2); pin(aC3); pin(aR2); pin(ou); pin(mean_t); pin(rstd_t);
    f32x4 acc[2][4], cse[1][2];
    using F = Frag<2, 4>;
    F f[2];
    auto mm = [&](const F& g, bool first) {
#pragma unroll
      for (int i = 0; i < 4; ++i) {
#pragma unroll
        for (int mt = 0; mt < 2; ++mt) acc[mt][i] = mfma0(first, g.a[mt], g.b[i], acc[mt][i]);
        if (i < 2) cse[0][i] = mfma0(first, ones, g.b[i], cse[0][i]);
      }
    };
#pragma unroll
    for (int v = 0; v < 16; ++v) {
      const int s2 = v >> 1, ks = v & 1;
      if (ks == 0) cbar<true>(4 + s2);
      if (v > 1) { pin_all(acc); pin_all(cse); SB(); }
#pragma unroll
      for (int mt = 0; mt < 2; ++mt) f[ks].a[mt] = lds_b128(lp(xa[v & 3]) + (s2 >> 1) * SLOT + mt * 4096);
#pragma unroll
      for (int i = 0; i < 4; ++i) f[ks].b[i] = lds_b128(lp(bo[ks][i >> 1]) + (s2 & 3) * SLOT + (i & 1) * 2048);
      if (v > 0) {
        mm(f[ks ^ 1], v == 1);
        interleave<6, 10, 1>();
      }
      SB();
    }
    mm(f[1], false);
    *(LAS float*)lp(aC2) = cse[0][0][0];
    *(LAS float*)(lp(aC2) + 64) = cse[0][1][0];
    cbar<true>(85);
    {
      const float cs[4] = {cse[0][0][0], cse[0][1][0], *(LAS float*)lp(aC3), *(LAS float*)(lp(aC3) + 64)};
#pragma unroll
      for (int i = 0; i < 4; ++i) {
#pragma unroll
        for (int mt = 0; mt < 2; ++mt) {
          const f32x4 uu = acc[mt][i] * rd[mt] + mr[mt] * cs[i];
          *(LAS u32x2*)(lp(aW2[i >> 1][mt]) + (i & 1) * 1024) = u32x2{pk2(lo2(uu)), pk2(hi2(uu))};
        }
      }
#pragma unroll
      for (int ii = 0; ii < 4; ++ii)
        __builtin_amdgcn_raw_buffer_store_b128(*(LAS u32x4*)(lp(aR2) + 1024 * ii), rs, ou + ii * 8 * 8192, 0, 16);
    }
#pragma unroll
    for (int j = 12; j < 20; ++j) {
      if (j > 13) lazy_pub(false, 1, WS_F2 + (c * 32 + r) * FS);
      cbar<true>(j);
    }
    cbar<false>(86);
    lazy_pub(true, 1, WS_F2 + (c * 32 + r) * FS);
    scan_setup();
  }

  SB();
  pin(xq[0]); pin(xq[1]); pin(mean_t); pin(rstd_t);
  int lane2 = lane;
  pin(lane2);
  const int fr2 = lane2 & 15, q2 = lane2 >> 4, tq2 = fr2 >> 2, tp2 = fr2 & 3;
  u32x2 dk[2];
#pragma unroll
  for (int i = 0; i < 2; ++i) dk[i] = *(const u32x2*)(p.dskip + (64 * c + 32 * np + 4 * q2 + 16 * i) * 2);
  int aSnd = lb + L_STG + cw * 2048 + lane2 * 16, aRcv = lb + L_STG + (cw ^ 4) * 2048 + lane2 * 16;
  pin(aSnd); pin(aRcv);
  f32x4 accS[2][2];
  int a6[6];
#pragma unroll
  for (int i = 0; i < 2; ++i) {
    const int row = 32 * np + 16 * i + fr2;
    a6[4 + i] = lb + 8192 + row * 128 + (((4 * kh + q2) ^ ((row >> 1) & 7)) * 16);
    pin(a6[4 + i]);
#pragma unroll
    for (int t = 0; t < 2; ++t) {
      const int rn = 8 * q2 + 4 * t + tq2;
      a6[2 * i + t] = lb + kh * 4096 + rn * 128 + (((2 * t2 + (i ^ kh)) ^ hswz(rn)) * 32) + tp2 * 8;
      pin(a6[2 * i + t]);
    }
  }
  const int n5 = cw & 3, th = cw >> 2;
  int a5[5];
#pragma unroll
  for (int t = 0; t < 2; ++t) {
    const int rp = 16 * t + 4 * q2 + tq2;
#pragma unroll
    for (int i = 0; i < 2; ++i) {
      a5[2 * i + t] = lb + 8192 + rp * 256 + (((2 * n5 + (i ^ th)) ^ (rp & 7)) * 32) + tp2 * 8;
      pin(a5[2 * i + t]);
    }
  }
  int aC5 = lb + L_P1 + (32 * n5 + 16 * th + 4 * q2) * 4;
  pin(aC5);
  a5[4] = lb + 4 * th * 1024 + lane2 * 16;
  int aS5 = lb + L_ST5 + (64 * th + fr2) * 4;
  unsigned oh5 = WS_HID + ((2 * R5 + th) * 56 + 4 * c5 + n5) * 4096 + lane2 * 16;
  pin(a5[4]); pin(aS5); pin(oh5);
  u32x4 orr, oi;
  auto store_h = [&] {
    __builtin_amdgcn_raw_buffer_store_b128(orr, rs, WS_HT + ch * 4096 + wv * 1024 + lane * 16, 0, 16);
    __builtin_amdgcn_raw_buffer_store_b128(oi, rs, WS_HT + (512 + ch) * 4096 + wv * 1024 + lane * 16, 0, 16);
  };
  {
    cbar<true>(20);
    if (kh) x1p = *(LAS u32x4*)(lds + L_STG + (cw ^ 4) * 2048 + lane * 16);
    LAS char* sl = lds + slot_of(20) + wv * 2048 + (lane >> 2) * 128 + chl * 64 + (lane & 3) * 16;
    const u32x4 ur = *(LAS u32x4*)sl, ui = *(LAS u32x4*)(sl + 8192);
    f32x2 v[8];
#pragma unroll
    for (int j = 0; j < 4; ++j) {
      v[2 * j] = f32x2{bf_lo(ur[j]), bf_lo(ui[j])};
      v[2 * j + 1] = f32x2{bf_hi(ur[j]), bf_hi(ui[j])};
    }
    f32x2 T = v[0];
#pragma unroll
    for (int j = 1; j < 8; ++j) T = cfma(a, T, v[j]);
    T = cfma(pw[0], dpp0c<0x111>(T), T);
    T = cfma(pw[1], dpp0c<0x112>(T), T);
    T = cfma(pw[2], dpp0c<0x114>(T), T);
    T = cfma(pw[3], dpp0c<0x118>(T), T);
    T = cfma(m16, dpp0c<0x142, 0xa>(T), T);
    T = cfma(m32, dpp0c<0x143, 0xc>(T), T);
    if (lane == 63) {
      *(LAS f32x2*)(lds + L_SCN + cw * 8) = T;
      asm volatile("" ::: "memory");
      *(volatile LAS int*)(lds + L_SF + 4 * cw) = 1;
    }
    f32x2 P = dpp0c<0x138>(T);
#pragma unroll 1
    for (unsigned spins = 0; spins < LDS_SPIN_CAP; ++spins) {
      const int fl = *(volatile LAS int*)(lds + L_SF + 4 * (4 * chl + (lane & 3)));
      if (__all(fl != 0 || (lane & 3) >= wv)) break;
    }
    asm volatile("" ::: "memory");
    f32x2 cin = {0.f, 0.f};
#pragma unroll
    for (int w = 0; w < 3; ++w) {
      const f32x2 tw = *(LAS f32x2*)(lds + L_SCN + (4 * chl + w) * 8);
      if (w < wv) cin = cfma(pw[6], cin, tw);
    }
    P = cfma(pl, cin, P);
    float hr[8], hi[8];
#pragma unroll
    for (int j = 0; j < 8; ++j) {
      P = cfma(a, P, v[j]);
      const f32x2 h = cmul(coef, P);
      hr[j] = h[0];
      hi[j] = -h[1];
    }
#pragma unroll
    for (int j = 0; j < 4; ++j) {
      orr[j] = pk2(f32x2{hr[2 * j], hr[2 * j + 1]});
      oi[j] = pk2(f32x2{hi[2 * j], hi[2 * j + 1]});
    }
    pin(orr); pin(oi);
    store_h();
  }

  pin(x1p);
  auto ld6 = [&](F22& f, const int (&ad)[6], int off) {
#pragma unroll
    for (int i = 0; i < 2; ++i) {
      f.a[i] = lds_b128(lp(ad[4 + i]) + off);
      f.b[i] = lds_tr2(lp(ad[2 * i]) + off, lp(ad[2 * i + 1]) + off);
    }
  };
  auto mm6 = [&](const F22& f, bool first) {
#pragma unroll
    for (int i = 0; i < 2; ++i)
#pragma unroll
      for (int m = 0; m < 2; ++m) accS[i][m] = mfma0(first, f.a[i], f.b[m], accS[i][m]);
  };
  if (do5) {
    f32x4 acc[2][4], csa[1][1];
    using F = Frag<2, 4>;
    pipe<16, 21, 8, 9, 1, F>(a5,
      [&](F& f, const int (&ad)[5], int off) {
#pragma unroll
        for (int i = 0; i < 2; ++i) f.a[i] = lds_tr2(lp(ad[2 * i]) + off, lp(ad[2 * i + 1]) + off);
#pragma unroll
        for (int m = 0; m < 4; ++m) f.b[m] = lds_b128(lp(ad[4]) + off + m * 1024);
      },
      [&](const F& f, bool first) {
        csa[0][0] = mfma0(first, f.a[0], ones, csa[0][0]);
#pragma unroll
        for (int m = 0; m < 4; ++m)
#pragma unroll
          for (int i = 0; i < 2; ++i) acc[i][m] = mfma0(first, f.a[i], f.b[m], acc[i][m]);
      },
      [&] { pin_all(acc); pin_all(csa); },
      [&](int u) {
        if (u == -PUB4) {
          if (cw == 0 && lane2 < 8) st_flag(ws + f4 + lane2 * 2048, tag);
          SB();
        }
      });
    if (fr2 == 0) *(LAS f32x4*)lp(aC5) = csa[0][0];
    float g5[4][4];
#pragma unroll
    for (int m = 0; m < 4; ++m)
#pragma unroll
      for (int k = 0; k < 4; ++k) g5[m][k] = *(LAS float*)(lp(aS5) + 64 * m + 512 * k);
    cbar<true>(87);
    const f32x4 cs1 = *(LAS f32x4*)lp(aC5 ^ 64);
    SB();
#pragma unroll
    for (int m = 0; m < 4; ++m) {
      const u32x2 g0 = gelu_pk4(acc[0][m] + csa[0][0] * g5[m][0], g5[m][1], g5[m][2], g5[m][3]);
      const u32x2 g1 = gelu_pk4(acc[1][m] + cs1 * g5[m][0], g5[m][1], g5[m][2], g5[m][3]);
      __builtin_amdgcn_raw_buffer_store_b128(u32x4{g0[0], g0[1], g1[0], g1[1]}, rs, oh5 + m * 1024, 0, 16);
    }
    pipe<16, 37, 6, 4, 6, F22>(a6, ld6, mm6, [&] { pin_all(accS); }, [&](int u) {
      if (u == -PUB5) raise(WS_F5 + (R5 * 16 + c5) * FS);
    });
  } else {
    publish<8>(lb + L_CNT + 12, 8u, ws + f4, tag, lane2);
    __builtin_amdgcn_s_sleep(16);
    {
      const unsigned dump = lb + L_FLG + 2048, id = ((int)blockIdx.x - 224) * 8 + cw, vl = lane2 * 16;
#pragma unroll
      for (int k = 0; k < 7; ++k) warm(p.w4 + id * 7168 + k * 1024, vl, dump);
    }
    pipe<16, 37, 6, 4, 6, F22>(a6, ld6, mm6, [&] { pin_all(accS); }, [](int) {});
  }

  {
    int aWT[6];
#pragma unroll
    for (int t = 0; t < 2; ++t) {
      const int rp = 16 * (t ^ (r & 1)) + 4 * q2 + tq2;
#pragma unroll
      for (int i = 0; i < 2; ++i) aWT[2 * i + t] = lb + 8192 + kh * 4096 + rp * 128 + (((2 * np + i) ^ hswz(rp)) * 32) + tp2 * 8;
      aWT[4 + t] = lb + kh * 4096 + (2 * t2 + (t ^ kh)) * 1024 + lane2 * 16;
    }
#pragma unroll
    for (int i = 0; i < 6; ++i) pin(aWT[i]);
    auto ld_wt = [&](F22& f, const int (&ad)[6], int off) {
#pragma unroll
      for (int i = 0; i < 2; ++i) {
        f.a[i] = lds_tr2(lp(ad[2 * i]) + off, lp(ad[2 * i + 1]) + off);
        f.b[i] = lds_b128(lp(ad[4 + i]) + off);
      }
    };
    const auto ro = __builtin_amdgcn_make_buffer_rsrc(p.out, 0, SEQ * DM * 4, 0x00020000);
    xch_send(aSnd, accS);
    f32x4 accH[2][2], sv[2], zv[2], xv[2];
    int aW, aR;
    unsigned po;
    pipe<28, 53, 6, 4, 6, F22>(aWT, ld_wt,
      [&](const F22& f, bool first) {
#pragma unroll
        for (int i = 0; i < 2; ++i)
#pragma unroll
          for (int m = 0; m < 2; ++m) accH[i][m] = mfma0(first, f.a[i], f.b[m], accH[i][m]);
      },
      [&] { pin_all(accH); },
      [&](int u) {
        if (u == 2 || u == 3) {
          const int i = u & 1;
          const f32x2 x0 = unpk(xq[i][0]), x1 = unpk(xq[i][1]), d0 = unpk(dk[i][0]), d1 = unpk(dk[i][1]);
          xv[i] = f32x4{x0[0], x0[1], x1[0], x1[1]};
          sv[i] = (xv[i] - mean_t) * rstd_t * f32x4{d0[0], d0[1], d1[0], d1[1]} + accS[i][0] +
                  *(LAS f32x4*)(lp(aRcv) + 1024 * i);
          pin(sv[i]); pin(xv[i]);
        }
        if (u == 4) {
#pragma unroll
          for (int i = 0; i < 2; ++i) {
            const f32x2 z0 = unpk(x1p[2 * i]), z1 = unpk(x1p[2 * i + 1]);
            zv[i] = f32x4{z0[0], z0[1], z1[0], z1[1]};
            pin(zv[i]);
          }
        }
        if (u == 5) {
          aW = lb + L_STG + (cw ^ 4) * 2048 + fr2 * 128 + ((q2 ^ (fr2 & 7)) * 16);
          aR = lb + L_STG + (cw ^ 4) * 2048 + (lane2 >> 3) * 128 + (((lane2 & 7) ^ (lane2 >> 3)) * 16);
          po = ((64 * r + 16 * tt + (lane2 >> 3)) * DM + 64 * c + 32 * np + (lane2 & 7) * 4) * 4;
          pin(aW); pin(aR); pin(po);
        }
      });
    xch_send(aSnd, accH);
    zv[0] += accH[0][0];
    zv[1] += accH[1][0];
    cbar<true>();
    const f32x4 r0 = *(LAS f32x4*)lp(aRcv), r1 = *(LAS f32x4*)(lp(aRcv) + 1024);
    SB();
    zv[0] += r0;
    zv[1] += r1;
#pragma unroll
    for (int i = 0; i < 2; ++i) {
      const f32x2 o0 = lo2(sv[i]) * silu2(lo2(zv[i])) + lo2(xv[i]);
      const f32x2 o1 = hi2(sv[i]) * silu2(hi2(zv[i])) + hi2(xv[i]);
      *(LAS f32x4*)lp(aW ^ (64 * i)) = f32x4{o0[0], o0[1], o1[0], o1[1]};
    }
#pragma unroll
    for (int ii = 0; ii < 2; ++ii)
      __builtin_amdgcn_raw_buffer_store_b128(*(LAS u32x4*)(lp(aR) + 1024 * ii), ro, po + ii * 8 * DM * 4, 0, 16);
  }
}

__global__ void __launch_bounds__(768, 3) fused_kernel(Params p) {
  extern __shared__ __attribute__((aligned(16))) char smem[];
  LAS char* lds = (LAS char*)smem;
  const char* code;
  asm volatile("s_getpc_b64 %0" : "=s"(code));
  const int wave = __builtin_amdgcn_readfirstlane(threadIdx.x >> 6), lane = threadIdx.x & 63;
  const int g = blockIdx.x & 7, jx = blockIdx.x >> 3;
  if (wave < 2)
    warm((const char*)((size_t)code & ~(size_t)255) + jx * CODE_CH + wave * (CODE_CH - 1024), lane * 16,
         (unsigned)(size_t)lds + L_FLG + 2048);
  const int r = (g >> 1) * 8 + (jx >> 2), c = (g & 1) * 4 + (jx & 3);
  const bool do5 = jx < 28;
  const int R5 = (g >> 1) * 4 + jx / 7, c5 = (g & 1) * 7 + jx % 7;
  if (wave >= 8) loader(p, lds, wave - 8, lane, r, c, R5, c5, do5);
  else compute(p, lds, wave, lane, r, c, R5, c5, do5);
}
}

extern "C" void kernel_launch(void* const* d_in, const int* in_sizes, int n_in, void* d_out, int out_size,
                              void* d_ws, size_t ws_size, hipStream_t stream) {
  (void)in_sizes; (void)n_in; (void)out_size; (void)ws_size;
  Params p;
  p.x = (const char*)d_in[0];
  p.lam_re = (const char*)d_in[1];
  p.lam_im = (const char*)d_in[2];
  p.b_re = (const char*)d_in[3];
  p.b_im = (const char*)d_in[4];
  p.c_re = (const char*)d_in[5];
  p.c_im = (const char*)d_in[6];
  p.log_dt = (const char*)d_in[7];
  p.dskip = (const char*)d_in[8];
  p.w1 = (const char*)d_in[10];
  p.w2 = (const char*)d_in[11];
  p.w3 = (const char*)d_in[12];
  p.w4 = (const char*)d_in[13];
  p.out = (float*)d_out;
  p.ws = (char*)d_ws;
  (void)hipFuncSetAttribute((const void*)fused_kernel, hipFuncAttributeMaxDynamicSharedMemorySize, LDS_BYTES);
  fused_kernel<<<256, 768, LDS_BYTES, stream>>>(p);
}
```
